```python
import torch

def _softmax(x, axis=-1):
    return torch.softmax(x, dim=axis)

def _gelu(x):
    return torch.nn.functional.gelu(x, approximate='tanh')

def _silu(x):
    return torch.nn.functional.silu(x)

def _sigmoid(x):
    return torch.sigmoid(x)

def _layernorm(x, axis=-1, eps=1e-05):
    m = x.mean(dim=axis, keepdim=True)
    v = x.var(dim=axis, keepdim=True, unbiased=False)
    return (x - m) / torch.sqrt(v + eps)

def _scatter_add(tgt, idx, src, axis=0):
    return tgt.clone().index_add_(axis, idx, src)

def _segment_sum(src, seg, n):
    out = torch.zeros((n,) + src.shape[1:], dtype=src.dtype)
    return out.index_add_(0, seg, src)

def _pad(x, pw):
    flat = tuple((v for pair in reversed(pw) for v in pair))
    return torch.nn.functional.pad(x, flat)
kernel_name = 'interleave_layers__softmax_full_mha__memory_aug_recurrent_mem_tr__prompt'

def setup_inputs():

    _w = lambda a: (lambda t: t.to(torch.bfloat16) if t.is_floating_point() else t)(torch.as_tensor(a))

    def _A_setup():
        B, S, D = (1, 2048, 512)
        return {'x': _w((lambda *s: torch.randn(*s, dtype=torch.bfloat16) * 0.05)(B, S, D)), 'wqkv': _w((lambda *s: torch.randn(*s, dtype=torch.bfloat16) * 0.05)(D, 3 * D)), 'wo': _w((lambda *s: torch.randn(*s, dtype=torch.bfloat16) * 0.05)(D, D))}
    _A_in = _A_setup()

    def _B_setup():
        B, S, Dm = (1, 2048, 512)
        H, Dh = (32, 128)
        M, seg = (8, 32)
        seg = min(seg, S)
        S = S // seg * seg
        return {'x': _w((lambda *s: torch.randn(*s, dtype=torch.bfloat16) * 0.05)(B, S, Dm)), 'mem0': _w((lambda *s: torch.randn(*s, dtype=torch.bfloat16) * 0.8)(B, M, Dm)), 'Wq': _w((lambda *s: torch.randn(*s, dtype=torch.bfloat16) * 1.6)(Dm, H, Dh) * Dm ** (-0.5)), 'Wk': _w((lambda *s: torch.randn(*s, dtype=torch.bfloat16) * 1.6)(Dm, H, Dh) * Dm ** (-0.5)), 'Wv': _w((lambda *s: torch.randn(*s, dtype=torch.bfloat16) * 0.8)(Dm, H, Dh) * Dm ** (-0.5)), 'Wo': _w((lambda *s: torch.randn(*s, dtype=torch.bfloat16) * 0.8)(H, Dh, Dm) * (H * Dh) ** (-0.5)), 'seg_len': seg}
    _B_in = _B_setup()
    return {'A_x': _A_in['x'], 'A_wqkv': _A_in['wqkv'], 'A_wo': _A_in['wo'], 'B_x': _B_in['x'], 'B_mem0': _B_in['mem0'], 'B_Wq': _B_in['Wq'], 'B_Wk': _B_in['Wk'], 'B_Wv': _B_in['Wv'], 'B_Wo': _B_in['Wo'], 'B_seg_len': _B_in['seg_len']}

def reference(A_x, A_wqkv, A_wo, B_x, B_mem0, B_Wq, B_Wk, B_Wv, B_Wo, B_seg_len):

    def _A_ref(x, wqkv, wo):
        B, S, D = x.shape
        H = 32
        Dh = D // H
        qkv = (lambda x, *d: x.permute(*d))((x @ wqkv).reshape(B, S, 3, H, Dh), 2, 0, 3, 1, 4)
        q, k, v = (qkv[0], qkv[1], qkv[2])
        scale = Dh ** (-0.5)
        scores = torch.einsum('bhqd,bhkd->bhqk', q, k) * scale
        p = _softmax(scores, axis=-1)
        o = torch.einsum('bhqk,bhkd->bhqd', p, v)
        return (lambda x, *d: x.permute(*d))(o, 0, 2, 1, 3).reshape(B, S, D) @ wo

    def _B_ref(x, mem0, Wq, Wk, Wv, Wo, seg_len):
        B, S, Dm = x.shape
        H, Dh = (Wq.shape[1], Wq.shape[2])
        M = mem0.shape[1]
        scale = Dh ** (-0.5)
        n_seg = S // seg_len
        L = M + seg_len
        idx = torch.arange(L)
        mask = (idx[:, None] < M) | (idx[:, None] >= idx[None, :])
        mem = mem0
        outs = []
        for s in range(n_seg):
            xs = x[:, s * seg_len:(s + 1) * seg_len, :].clone()
            h = torch.concatenate([mem, xs], axis=1)
            q = torch.einsum('btd,dhe->bhte', h, Wq)
            k = torch.einsum('btd,dhe->bhte', h, Wk)
            v = torch.einsum('btd,dhe->bhte', h, Wv)
            scores = torch.einsum('bhqe,bhke->bhqk', q, k) * scale
            scores = torch.where(mask[None, None], scores, -1000000000.0)
            p = _softmax(scores, axis=-1)
            o = torch.einsum('bhqk,bhke->bhqe', p, v)
            o = torch.einsum('bhte,hed->btd', o, Wo)
            mem = o[:, :M, :].clone()
            outs.append(o[:, M:, :])
        return torch.concatenate(outs, axis=1)
    x = A_x
    x = x + _A_ref(_layernorm(x, axis=-1), A_wqkv, A_wo)
    x = x + _B_ref(_layernorm(x, axis=-1), B_mem0, B_Wq, B_Wk, B_Wv, B_Wo, B_seg_len)
    x = x + _A_ref(_layernorm(x, axis=-1), A_wqkv, A_wo)
    x = x + _B_ref(_layernorm(x, axis=-1), B_mem0, B_Wq, B_Wk, B_Wv, B_Wo, B_seg_len)
    return x
```

```cpp
#include <hip/hip_runtime.h>
#include <hip/hip_bf16.h>
#include <stdint.h>

typedef unsigned short u16;
typedef __bf16 bf16x8 __attribute__((ext_vector_type(8)));
typedef __bf16 bf16x16 __attribute__((ext_vector_type(16)));
typedef short s16x4 __attribute__((ext_vector_type(4)));
typedef short s16x8 __attribute__((ext_vector_type(8)));
typedef float f32x4 __attribute__((ext_vector_type(4)));
typedef float f32x16 __attribute__((ext_vector_type(16)));
typedef unsigned u32x4 __attribute__((ext_vector_type(4)));
typedef unsigned u32x2 __attribute__((ext_vector_type(2)));
typedef unsigned u32x8 __attribute__((ext_vector_type(8)));
#define LAS __attribute__((address_space(3)))
#define GAS __attribute__((address_space(1)))
typedef GAS unsigned gu32;
#define RLX_AGENT __ATOMIC_RELAXED, __HIP_MEMORY_SCOPE_AGENT
#define DI __device__ __forceinline__

constexpr int S = 2048, D = 512, NSEG = 64;
constexpr size_t MB = 1u << 20;
constexpr size_t OFF_CTL = 0;
constexpr size_t OFF_XA = 1 * MB;
constexpr size_t OFF_XB = 5 * MB;
constexpr size_t OFF_XN = 9 * MB;
constexpr size_t OFF_QA = 11 * MB;
constexpr size_t OFF_KA = 13 * MB;
constexpr size_t OFF_VA = 15 * MB;
constexpr size_t OFF_OA = 17 * MB;
constexpr size_t OFF_P = 19 * MB;
constexpr size_t OFF_M = 20 * MB;
constexpr size_t OFF_KX = 24 * MB;
constexpr size_t OFF_VX = 40 * MB;
constexpr size_t OFF_QX = 56 * MB;
constexpr size_t OFF_XF = 72 * MB;
constexpr size_t OFF_VM = 80 * MB;
constexpr size_t OFF_OB = 88 * MB;

constexpr float LOG2E = 1.4426950408889634f;
constexpr float CA = 0.25f * LOG2E;
constexpr float CB = 0.08838834764831845f * LOG2E;

typedef __bf16 bf16x2 __attribute__((ext_vector_type(2)));
typedef float f32x2 __attribute__((ext_vector_type(2)));
DI unsigned cvtpk(float lo, float hi) { f32x2 v = {lo, hi}; return __builtin_bit_cast(unsigned, __builtin_convertvector(v, bf16x2)); }
DI float bflo(unsigned v) { return __uint_as_float(v << 16); }
DI float bfhi(unsigned v) { return __uint_as_float(v & 0xffff0000u); }
DI s16x4 trread(const LAS char* p) { return __builtin_amdgcn_ds_read_tr16_b64_v4i16((LAS s16x4*)p); }
DI bf16x8 cat8(s16x4 a, s16x4 b) { s16x8 r = {a[0], a[1], a[2], a[3], b[0], b[1], b[2], b[3]}; return __builtin_bit_cast(bf16x8, r); }
DI bf16x8 asbf8(u32x4 v) { return __builtin_bit_cast(bf16x8, v); }
DI u32x4 asu4(bf16x8 v) { return __builtin_bit_cast(u32x4, v); }
DI s16x4 ass4(u32x2 v) { return __builtin_bit_cast(s16x4, v); }
#define MFMA16(a, b, c) __builtin_amdgcn_mfma_f32_16x16x32_bf16(a, b, c, 0, 0, 0)
#define MFMA32(a, b, c) __builtin_amdgcn_mfma_f32_32x32x16_bf16(a, b, c, 0, 0, 0)
#define MFMA16K16(a, b, c) __builtin_amdgcn_mfma_f32_16x16x16bf16_1k(a, b, c, 0, 0, 0)
#define SMF(a, b, c, idx) __builtin_amdgcn_smfmac_f32_16x16x64_bf16(a, b, c, idx, 0, 0)
#define LBAR() asm volatile("s_waitcnt lgkmcnt(0)\n\ts_barrier" ::: "memory")
DI float swap32_add(float a, float b) { auto r = __builtin_amdgcn_permlane32_swap(__float_as_uint(a), __float_as_uint(b), false, false); return __uint_as_float(r[0]) + __uint_as_float(r[1]); }
DI float xmax32(float a) { auto r = __builtin_amdgcn_permlane32_swap(__float_as_uint(a), __float_as_uint(a), false, false); return fmaxf(__uint_as_float(r[0]), __uint_as_float(r[1])); }
DI float xmax16(float a) { auto r = __builtin_amdgcn_permlane16_swap(__float_as_uint(a), __float_as_uint(a), false, false); return fmaxf(__uint_as_float(r[0]), __uint_as_float(r[1])); }
DI float xsum32(float a) { auto r = __builtin_amdgcn_permlane32_swap(__float_as_uint(a), __float_as_uint(a), false, false); return __uint_as_float(r[0]) + __uint_as_float(r[1]); }
DI float xsum16(float a) { auto r = __builtin_amdgcn_permlane16_swap(__float_as_uint(a), __float_as_uint(a), false, false); return __uint_as_float(r[0]) + __uint_as_float(r[1]); }

struct Params {
    const u16 *x, *wqkv, *wo, *mem0, *Wq, *Wk, *Wv, *Wo;
    u16* out;
    char* ws;
};

template <bool IN_BF16>
__global__ void __launch_bounds__(256) k_ln(const void* in, u16* out) {
    const int row = blockIdx.x * 4 + (threadIdx.x >> 6), lane = threadIdx.x & 63;
    float v[8];
    if (IN_BF16) { u32x4 u = *(const u32x4*)((const u16*)in + (size_t)row * D + lane * 8);
        for (int i = 0; i < 4; ++i) { v[2 * i] = bflo(u[i]); v[2 * i + 1] = bfhi(u[i]); } }
    else { const float* p = (const float*)in + (size_t)row * D + lane * 8; f32x4 a = *(const f32x4*)p, b = *(const f32x4*)(p + 4);
        for (int i = 0; i < 4; ++i) { v[i] = a[i]; v[4 + i] = b[i]; } }
    float s = 0; for (int i = 0; i < 8; ++i) s += v[i];
    for (int m = 1; m < 64; m <<= 1) s += __shfl_xor(s, m);
    const float mean = s * (1.0f / D);
    float q = 0; for (int i = 0; i < 8; ++i) { v[i] -= mean; q += v[i] * v[i]; }
    for (int m = 1; m < 64; m <<= 1) q += __shfl_xor(q, m);
    const float rs = 1.0f / sqrtf(q * (1.0f / D) + 1e-5f);
    u32x4 o; for (int i = 0; i < 4; ++i) o[i] = cvtpk(v[2 * i] * rs, v[2 * i + 1] * rs);
    *(u32x4*)(out + (size_t)row * D + lane * 8) = o;
}

constexpr int GA_STR = 144;
constexpr int GW_STR = 272;
template <int MODE>
__global__ void __launch_bounds__(256) k_gemm(Params p, int layer) {
    __shared__ __attribute__((aligned(16))) char sm[128 * GA_STR + 64 * GW_STR];
    LAS char* sA = (LAS char*)sm; LAS char* sW = sA + 128 * GA_STR;
    const int tid = threadIdx.x, lane = tid & 63, w = tid >> 6, c = lane & 15, g = lane >> 4;
    const int m0 = blockIdx.y * 128; int n0 = blockIdx.x * 128;
    const u16 *A, *W; int lda, ldw, K, mat = 0;
    if (MODE == 0) { A = (const u16*)(p.ws + OFF_XN); lda = D; W = p.wqkv; ldw = 1536; K = D; }
    else if (MODE == 1) { A = (const u16*)(p.ws + OFF_OA); lda = D; W = p.wo; ldw = D; K = D; }
    else if (MODE == 2) { A = (const u16*)(p.ws + OFF_XN); lda = D; mat = blockIdx.x / 32; n0 = (blockIdx.x % 32) * 128; W = mat == 0 ? p.Wq : (mat == 1 ? p.Wk : p.Wv); ldw = 4096; K = D; }
    else { A = (const u16*)(p.ws + OFF_OB); lda = 4096; W = p.Wo; ldw = D; K = 4096; }
    bool tr;
    if (MODE == 0) tr = n0 < 1024; else if (MODE == 1 || MODE == 3) tr = true; else tr = (mat != 2);
    const int wm = (w >> 1) * 64, wn = (w & 1) * 64;
    f32x4 acc[4][4];
    for (int i = 0; i < 4; ++i) for (int j = 0; j < 4; ++j) acc[i][j] = (f32x4){0, 0, 0, 0};
    for (int k0 = 0; k0 < K; k0 += 64) {
        u32x4 ra[4], rw[4];
#pragma unroll
        for (int i = 0; i < 4; ++i) { const int id = tid + i * 256; ra[i] = *(const u32x4*)(A + (size_t)(m0 + (id >> 3)) * lda + k0 + (id & 7) * 8);
            rw[i] = *(const u32x4*)(W + (size_t)(k0 + (id >> 4)) * ldw + n0 + (id & 15) * 8); }
        __syncthreads();
#pragma unroll
        for (int i = 0; i < 4; ++i) { const int id = tid + i * 256; *(LAS u32x4*)(sA + (id >> 3) * GA_STR + (id & 7) * 16) = ra[i]; *(LAS u32x4*)(sW + (id >> 4) * GW_STR + (id & 15) * 16) = rw[i]; }
        __syncthreads();
#pragma unroll
        for (int kk = 0; kk < 2; ++kk) {
            bf16x8 af[4], wf[4];
#pragma unroll
            for (int i = 0; i < 4; ++i) af[i] = asbf8(*(const LAS u32x4*)(sA + (wm + i * 16 + c) * GA_STR + kk * 64 + g * 16));
#pragma unroll
            for (int j = 0; j < 4; ++j) { const LAS char* q = sW + (kk * 32 + 8 * g + (c >> 2)) * GW_STR + (wn + j * 16 + (c & 3) * 4) * 2; wf[j] = cat8(trread(q), trread(q + 4 * GW_STR)); }
            if (tr) {
#pragma unroll
                for (int i = 0; i < 4; ++i)
#pragma unroll
                    for (int j = 0; j < 4; ++j) acc[i][j] = MFMA16(wf[j], af[i], acc[i][j]);
            } else {
#pragma unroll
                for (int i = 0; i < 4; ++i)
#pragma unroll
                    for (int j = 0; j < 4; ++j) acc[i][j] = MFMA16(af[i], wf[j], acc[i][j]);
            }
        }
    }
    if (MODE == 0) {
#pragma unroll
        for (int i = 0; i < 4; ++i)
#pragma unroll
            for (int j = 0; j < 4; ++j) {
                const int n = n0 + wn + j * 16, which = n >> 9, h = (n & 511) >> 4;
                if (which < 2) {
                    const int row = m0 + wm + i * 16 + c; const float sc = which == 0 ? CA : 1.0f;
                    u32x2 v = {cvtpk(acc[i][j][0] * sc, acc[i][j][1] * sc), cvtpk(acc[i][j][2] * sc, acc[i][j][3] * sc)};
                    *(u32x2*)(p.ws + (which == 0 ? OFF_QA : OFF_KA) + (((size_t)h * S + row) * 16 + 4 * g) * 2) = v;
                } else {
                    const int key0 = m0 + wm + i * 16;
                    u32x2 v = {cvtpk(acc[i][j][0], acc[i][j][1]), cvtpk(acc[i][j][2], acc[i][j][3])};
                    *(u32x2*)(p.ws + OFF_VA + (size_t)h * 65536 + (key0 >> 4) * 512 + ((g & 1) * 16 + c) * 16 + (g >> 1) * 8) = v;
                }
            }
    } else if (MODE == 1 || MODE == 3) {
#pragma unroll
        for (int i = 0; i < 4; ++i)
#pragma unroll
            for (int j = 0; j < 4; ++j) {
                const int row = m0 + wm + i * 16 + c, col = n0 + wn + j * 16 + 4 * g;
                f32x4 r;
                if (MODE == 1 && layer == 0) { u32x2 u = *(const u32x2*)(p.x + (size_t)row * D + col); r = (f32x4){bflo(u[0]), bfhi(u[0]), bflo(u[1]), bfhi(u[1])}; }
                else r = *(const f32x4*)(p.ws + (MODE == 1 ? OFF_XB : OFF_XA) + ((size_t)row * D + col) * 4);
                r += acc[i][j];
                if (MODE == 3 && layer == 1) { u32x2 v = {cvtpk(r[0], r[1]), cvtpk(r[2], r[3])}; *(u32x2*)(p.out + (size_t)row * D + col) = v; }
                else *(f32x4*)(p.ws + (MODE == 1 ? OFF_XA : OFF_XB) + ((size_t)row * D + col) * 4) = r;
            }
    } else {
        const int h = n0 >> 7;
        if (mat != 2) {
            char* base = p.ws + (mat == 0 ? OFF_QX : OFF_KX);
            const float sc = mat == 0 ? CB : 1.0f;
#pragma unroll
            for (int i = 0; i < 4; ++i)
#pragma unroll
                for (int jj = 0; jj < 2; ++jj) {
                    const int row = m0 + wm + i * 16, seg = row >> 5, tau = (row >> 4) & 1, ks = (wn >> 5) + jj;
                    u32x4 v = {cvtpk(acc[i][2 * jj][0] * sc, acc[i][2 * jj][1] * sc), cvtpk(acc[i][2 * jj][2] * sc, acc[i][2 * jj][3] * sc),
                               cvtpk(acc[i][2 * jj + 1][0] * sc, acc[i][2 * jj + 1][1] * sc), cvtpk(acc[i][2 * jj + 1][2] * sc, acc[i][2 * jj + 1][3] * sc)};
                    *(u32x4*)(base + ((size_t)seg * 32 + h) * 8192 + (tau * 4 + ks) * 1024 + lane * 16) = v;
                }
        } else {
#pragma unroll
            for (int ii = 0; ii < 2; ++ii)
#pragma unroll
                for (int j = 0; j < 4; ++j) {
                    const int seg = (m0 + wm + ii * 32) >> 5, et = (wn >> 4) + j;
                    u32x4 v = {cvtpk(acc[2 * ii][j][0], acc[2 * ii][j][1]), cvtpk(acc[2 * ii][j][2], acc[2 * ii][j][3]),
                               cvtpk(acc[2 * ii + 1][j][0], acc[2 * ii + 1][j][1]), cvtpk(acc[2 * ii + 1][j][2], acc[2 * ii + 1][j][3])};
                    *(u32x4*)(p.ws + OFF_VX + ((size_t)seg * 32 + h) * 8192 + et * 1024 + lane * 16) = v;
                }
        }
    }
}

DI void attn_pass(const LAS char* sK, const LAS char* sV, bf16x8 qf, float negm, int lane, f32x16& o) {
    const int r = lane & 31, hi = lane >> 5;
    f32x16 cm; for (int i = 0; i < 16; ++i) cm[i] = negm;
    for (int i = 0; i < 16; ++i) o[i] = 0.f;
    const unsigned one2 = r == 16 ? 0x3f803f80u : 0u;
    u32x4 v0 = {one2, one2, one2, one2}, v1 = v0;
    const LAS char* kp = sK + r * 32 + hi * 16;
    const LAS char* vp = sV + (hi * 16 + (r & 15)) * 16;
    for (int T = 0; T < 64; ++T) {
        bf16x8 kf = asbf8(*(const LAS u32x4*)(kp + T * 1024));
        if (r < 16) { v0 = *(const LAS u32x4*)(vp + T * 1024); v1 = *(const LAS u32x4*)(vp + T * 1024 + 512); }
        f32x16 s = MFMA32(kf, qf, cm);
        u32x4 p0, p1;
#pragma unroll
        for (int i = 0; i < 4; ++i) { p0[i] = cvtpk(__builtin_amdgcn_exp2f(s[2 * i]), __builtin_amdgcn_exp2f(s[2 * i + 1]));
                                      p1[i] = cvtpk(__builtin_amdgcn_exp2f(s[8 + 2 * i]), __builtin_amdgcn_exp2f(s[8 + 2 * i + 1])); }
        o = MFMA32(asbf8(v0), asbf8(p0), o);
        o = MFMA32(asbf8(v1), asbf8(p1), o);
    }
}
DI float attn_truemax(const LAS char* sK, bf16x8 qf, int lane) {
    const int r = lane & 31, hi = lane >> 5;
    f32x16 z; for (int i = 0; i < 16; ++i) z[i] = 0.f;
    float m = -INFINITY;
    const LAS char* kp = sK + r * 32 + hi * 16;
    for (int T = 0; T < 64; ++T) {
        bf16x8 kf = asbf8(*(const LAS u32x4*)(kp + T * 1024));
        f32x16 s = MFMA32(kf, qf, z);
        for (int i = 0; i < 16; ++i) m = fmaxf(m, s[i]);
    }
    return xmax32(m);
}
__global__ void __launch_bounds__(256) k_attnA(Params p) {
    extern __shared__ __attribute__((aligned(16))) char dsm[];
    LAS char* sK = (LAS char*)dsm; LAS char* sV = sK + 65536; LAS float* red = (LAS float*)(sV + 65536);
    const int h = blockIdx.x & 31, qb = blockIdx.x >> 5;
    const int tid = threadIdx.x, lane = tid & 63, w = tid >> 6, r = lane & 31, hi = lane >> 5;
    float kmax = 0.f;
    for (int i = tid; i < 4096; i += 256) {
        u32x4 u = *(const u32x4*)(p.ws + OFF_KA + (size_t)h * 65536 + i * 16); *(LAS u32x4*)(sK + i * 16) = u;
        *(LAS u32x4*)(sV + i * 16) = *(const u32x4*)(p.ws + OFF_VA + (size_t)h * 65536 + i * 16);
        float q = 0; for (int j = 0; j < 4; ++j) { float a = bflo(u[j]), b = bfhi(u[j]); q += a * a + b * b; }
        q += __shfl_xor(q, 1); kmax = fmaxf(kmax, q);
    }
    for (int m = 1; m < 64; m <<= 1) kmax = fmaxf(kmax, __shfl_xor(kmax, m));
    if (lane == 0) red[w] = kmax;
    __syncthreads();
    kmax = sqrtf(fmaxf(fmaxf(red[0], red[1]), fmaxf(red[2], red[3])));
    for (int t = 0; t < 2; ++t) {
        const int q0 = qb * 256 + w * 64 + t * 32;
        u32x4 qu = *(const u32x4*)(p.ws + OFF_QA + (((size_t)h * S + q0 + r) * 16 + hi * 8) * 2);
        float qq = 0; for (int j = 0; j < 4; ++j) { float a = bflo(qu[j]), b = bfhi(qu[j]); qq += a * a + b * b; }
        qq = xsum32(qq);
        float m = sqrtf(qq) * kmax;
        f32x16 o;
        attn_pass(sK, sV, asbf8(qu), -m, lane, o);
        float l = o[8]; l = __shfl(l, r);
        if (!__all(l >= 8.673617379884035e-19f)) {
            m = attn_truemax(sK, asbf8(qu), lane);
            attn_pass(sK, sV, asbf8(qu), -m, lane, o);
            l = o[8]; l = __shfl(l, r);
        }
        const float il = 1.0f / l;
        u16* op = (u16*)(p.ws + OFF_OA) + (size_t)(q0 + r) * D + h * 16;
        u32x2 a = {cvtpk(o[0] * il, o[1] * il), cvtpk(o[2] * il, o[3] * il)}, b = {cvtpk(o[4] * il, o[5] * il), cvtpk(o[6] * il, o[7] * il)};
        *(u32x2*)(op + 4 * hi) = a; *(u32x2*)(op + 8 + 4 * hi) = b;
    }
}

constexpr int RL_WV = 0;
constexpr int RL_M = 131072;
constexpr int RL_QK = RL_M + 8192;
constexpr int RL_O = RL_QK + 4096;
constexpr int RL_KX = RL_O + 2560;
constexpr int RL_END = RL_KX + 16384;
constexpr int RL_STAGE = RL_M;
constexpr int R_LDS = RL_END;
static_assert(R_LDS <= 163840, "lds");
constexpr unsigned SPIN_CAP = 1u << 20;
#define SBAR() __builtin_amdgcn_sched_barrier(0)

DI bf16x16 cat16(s16x4 a, s16x4 b, s16x4 c, s16x4 d) {
    typedef short s16x16 __attribute__((ext_vector_type(16)));
    s16x16 r = {a[0], a[1], a[2], a[3], b[0], b[1], b[2], b[3], c[0], c[1], c[2], c[3], d[0], d[1], d[2], d[3]};
    return __builtin_bit_cast(bf16x16, r);
}
DI bf16x16 cat16u(u32x4 a, u32x4 b) { u32x8 r = {a[0], a[1], a[2], a[3], b[0], b[1], b[2], b[3]}; return __builtin_bit_cast(bf16x16, r); }
DI bool anysent(u32x4 a, u32x4 b) {
    unsigned m = max(max(max(a[0], a[1]), max(a[2], a[3])), max(max(b[0], b[1]), max(b[2], b[3])));
    return m == 0xffffffffu;
}
DI unsigned nosent(unsigned v) { return min(v, 0xfffffffeu); }
DI void dma16(__amdgpu_buffer_rsrc_t r, unsigned voff, unsigned soff, unsigned ldsbase) {
    unsigned sv;
    asm volatile("s_mov_b32 %0, m0\n\ts_mov_b32 m0, %4\n\ts_nop 0\n\tbuffer_load_dwordx4 %1, %2, %3 offen sc1 lds\n\ts_mov_b32 m0, %0" : "=&s"(sv) : "v"(voff), "s"(r), "s"(soff), "s"(ldsbase) : "memory");
}

#define ASMF_A(acc, a, b, idx) asm volatile("v_smfmac_f32_16x16x64_bf16 %0, %1, %2, %3" : "+v"(acc) : "v"(a), "a"(b), "v"(idx))
#define ASMF_V(acc, a, b, idx) asm volatile("v_smfmac_f32_16x16x64_bf16 %0, %1, %2, %3" : "+v"(acc) : "v"(a), "v"(b), "v"(idx))
#define AMF0(d, a, b) asm volatile("v_mfma_f32_16x16x32_bf16 %0, %1, %2, 0" : "=&v"(d) : "v"(a), "v"(b))
#define AMF(d, a, b) asm volatile("v_mfma_f32_16x16x32_bf16 %0, %1, %2, %0" : "+v"(d) : "v"(a), "v"(b))
#define AMFK16(d, a, b) asm volatile("v_mfma_f32_16x16x16_bf16 %0, %1, %2, %0" : "+v"(d) : "v"(a), "v"(b))
#define NOP2() asm volatile("s_nop 1")
#define FENCE1(a) asm volatile("s_nop 7" : "+v"(a))
#define FENCE2(a, b) asm volatile("s_nop 7" : "+v"(a), "+v"(b))
#define FENCE3(a, b, c) asm volatile("s_nop 7" : "+v"(a), "+v"(b), "+v"(c))
#define FENCE4(a, b, c, d) asm volatile("s_nop 7" : "+v"(a), "+v"(b), "+v"(c), "+v"(d))
#define PIN(x) asm volatile("" : "+v"(x))
__global__ void __launch_bounds__(256, 1) k_recur(Params p, int layer) {
    extern __shared__ __attribute__((aligned(16))) char dsm[];
    LAS char* sm = (LAS char*)dsm;
    const int h = blockIdx.x;
    const int tid = threadIdx.x, lane = tid & 63, c = lane & 15, g = lane >> 4;
    const int w = __builtin_amdgcn_readfirstlane(tid >> 6);
    gu32* ctl = (gu32*)(p.ws + OFF_CTL);
    const __amdgpu_buffer_rsrc_t rws = __builtin_amdgcn_make_buffer_rsrc(p.ws, 0, 0x10000000, 0x00020000);
    const int q4 = c >> 2, p4 = c & 3;
    const unsigned l16 = lane * 16;
    {
        const u32x4 z = {~0u, ~0u, ~0u, ~0u};
        for (int i = 0; i < 2; ++i) for (int n = 0; n < 2; ++n) __builtin_amdgcn_raw_buffer_store_b128(z, rws, l16, OFF_P + i * 262144 + (h * 4 + w) * 2048 + n * 1024, 16);
        for (int n = 0; n < 4; ++n) __builtin_amdgcn_raw_buffer_store_b128(z, rws, l16, OFF_M + layer * 524288 + h * 16384 + w * 4096 + n * 1024, 16);
    }
    bf16x16 wo[8][2], wq[2][8], wk[2][8];
#pragma unroll
    for (int ks = 0; ks < 2; ++ks) {
        __syncthreads();
        for (int i = tid; i < 4096; i += 256) *(LAS u32x4*)(sm + i * 16) = *(const u32x4*)(p.Wo + ((size_t)h * 128 + ks * 64) * D + i * 8);
        __syncthreads();
#pragma unroll
        for (int t = 0; t < 8; ++t) {
            s16x4 f[4];
#pragma unroll
            for (int rho = 0; rho < 4; ++rho) {
                const int e = 32 * (rho >> 1) + 16 * (g >> 1) + 8 * (q4 & 1) + 4 * (g & 1) + 2 * (rho & 1) + (q4 >> 1);
                f[rho] = trread(sm + e * 1024 + (128 * w + 16 * t + 4 * p4) * 2);
            }
            wo[t][ks] = cat16(f[0], f[1], f[2], f[3]);
            asm volatile("" : "+v"(wo[t][ks]));
        }
    }
#pragma unroll
    for (int mat = 0; mat < 2; ++mat)
#pragma unroll
        for (int half = 0; half < 2; ++half) {
            __syncthreads();
            const u16* W = mat == 0 ? p.Wq : p.Wk;
            for (int i = tid; i < 4096; i += 256) *(LAS u32x4*)(sm + i * 16) = *(const u32x4*)(W + (size_t)(half * 256 + (i >> 4)) * 4096 + h * 128 + (i & 15) * 8);
            __syncthreads();
#pragma unroll
            for (int kq = 0; kq < 4; ++kq)
#pragma unroll
                for (int t = 0; t < 2; ++t) {
                    s16x4 f[4];
#pragma unroll
                    for (int rho = 0; rho < 4; ++rho) {
                        const int k = 64 * kq + 32 * (q4 & 1) + 16 * (g & 1) + 8 * (rho >> 1) + 4 * (g >> 1) + 2 * (rho & 1) + (q4 >> 1);
                        f[rho] = trread(sm + k * 256 + (32 * w + 16 * t + 4 * p4) * 2);
                    }
                    bf16x16 v = cat16(f[0], f[1], f[2], f[3]);
                    asm volatile("" : "+a"(v));
                    if (mat == 0) wq[t][half * 4 + kq] = v; else wk[t][half * 4 + kq] = v;
                }
        }
    for (int ks = 0; ks < 8; ++ks) {
        __syncthreads();
        for (int i = tid; i < 1024; i += 256) *(LAS u32x4*)(sm + RL_STAGE + i * 16) = *(const u32x4*)(p.Wv + (size_t)(ks * 64 + (i >> 4)) * 4096 + h * 128 + (i & 15) * 8);
        __syncthreads();
        for (int t = 0; t < 2; ++t) {
            s16x4 f[4];
#pragma unroll
            for (int rho = 0; rho < 4; ++rho) {
                const int k = 32 * (q4 & 1) + 16 * (g & 1) + 8 * (rho >> 1) + 4 * (g >> 1) + 2 * (rho & 1) + (q4 >> 1);
                f[rho] = trread(sm + RL_STAGE + k * 256 + (32 * w + 16 * t + 4 * p4) * 2);
            }
            LAS char* dst = sm + RL_WV + ((w * 2 + t) * 8 + ks) * 2048 + lane * 16;
            *(LAS u32x4*)dst = asu4(cat8(f[0], f[1])); *(LAS u32x4*)(dst + 1024) = asu4(cat8(f[2], f[3]));
        }
    }
    __syncthreads();
    for (int i = tid; i < 4096; i += 256) {
        const int row = i >> 9, k = i & 511;
        const int addr = (k >> 6) * 1024 + ((k >> 4) & 1) * 512 + ((k >> 3) & 1) * 256 + (row >> 2) * 128 + ((k >> 2) & 1) * 64 + (k & 3) * 16 + ((k >> 5) & 1) * 8 + (row & 3) * 2;
        *(LAS u16*)(sm + RL_M + addr) = p.mem0[i];
    }
    const unsigned ldsb = (unsigned)(uintptr_t)dsm;
    const unsigned kxs = OFF_KX + h * 8192 + w * 2048, vxs = OFF_VX + h * 8192 + w * 2048;
    u32x4 vx[2];
    for (int n = 0; n < 2; ++n) { dma16(rws, l16, kxs + n * 1024, ldsb + RL_KX + w * 2048 + n * 1024); vx[n] = __builtin_amdgcn_raw_buffer_load_b128(rws, l16, vxs + n * 1024, 16); }
    asm volatile("s_waitcnt vmcnt(0)" ::: "memory");
    __syncthreads();
    if (tid == 0) {
        __hip_atomic_fetch_add(ctl + 64 + layer * 64, 1u, RLX_AGENT);
        unsigned sp = 0;
        while (__hip_atomic_load(ctl + 64 + layer * 64, RLX_AGENT) < 32u) { __builtin_amdgcn_s_sleep(2); if (++sp > SPIN_CAP) { __hip_atomic_store(ctl, 1u, RLX_AGENT); break; } }
    }
    __syncthreads();

    const int idx = (c < 8) ? 0x8888 : 0xDDDD;
    const LAS char* mA = sm + RL_M + (p4 >> 1) * 8 + q4 * 16 + (g & 1) * 64 + (p4 & 1) * 128 + (g >> 1) * 256;
    const LAS char* wvp = sm + RL_WV + w * 32768 + lane * 16;
    const int e_w = 32 * w + 16 * (g >> 1) + c;
    const int sw = (e_w >> 2) & 3;
    LAS char* qw = sm + RL_QK + e_w * 32 + ((g & 1) ^ sw) * 8;
    LAS char* kw = sm + RL_QK + e_w * 32 + ((2 + (g & 1)) ^ sw) * 8;
    const LAS char* xr = sm + RL_QK + (4 * g + q4) * 32 + ((p4 ^ g) & 3) * 8;
    const LAS char* l16m = sm + RL_M + lane * 16;
    LAS char* ow = sm + RL_O + (c & 7) * 272 + (32 * w + 4 * g) * 2;
    const LAS char* orr = sm + RL_O + (c & 7) * 272 + (16 * g + 8 * (c >> 3)) * 2;
    u32x4 selu = {0, 0, 0, 0};
    if (c < 8) selu[c >> 1] = (c & 1) ? 0x3f800000u : 0x00003f80u;
    const bf16x8 sel = asbf8(selu);
    const unsigned pst = ((g >> 1) * 8 + ((c >> 3) & 1) * 4 + (g & 1) * 2 + ((c >> 2) & 1)) * 2048 + (c & 3) * 16;
    const unsigned mout = (c & 3) * 16 + (g & 1) * 8;
    bool fail = false;

    for (int s = 0; s < NSEG; ++s) {
        const int kb = s & 1;
        if (s + 1 < NSEG) { dma16(rws, l16, kxs + (s + 1) * 262144, ldsb + RL_KX + (kb ^ 1) * 8192 + w * 2048); dma16(rws, l16, kxs + (s + 1) * 262144 + 1024, ldsb + RL_KX + (kb ^ 1) * 8192 + w * 2048 + 1024); }
        bf16x8 af[8];
#pragma unroll
        for (int ks = 0; ks < 8; ++ks) af[ks] = cat8(trread(mA + ks * 1024), trread(mA + ks * 1024 + 512));
        f32x4 aq0 = {0, 0, 0, 0}, aq1 = aq0, ak0 = aq0, ak1 = aq0, av0 = aq0, av1 = aq0;
#pragma unroll
        for (int ks = 0; ks < 8; ++ks) { PIN(af[ks]); }
        PIN(aq0); PIN(aq1); PIN(ak0); PIN(ak1); PIN(av0); PIN(av1); NOP2();
#pragma unroll
        for (int ks = 0; ks < 8; ++ks) { ASMF_A(aq0, af[ks], wq[0][ks], idx); ASMF_A(aq1, af[ks], wq[1][ks], idx); ASMF_A(ak0, af[ks], wk[0][ks], idx); ASMF_A(ak1, af[ks], wk[1][ks], idx); }
        FENCE4(aq0, aq1, ak0, ak1);
        SBAR();
#define VSTEP(ks) do { bf16x16 b0_ = cat16u(*(const LAS u32x4*)(wvp + (ks) * 2048), *(const LAS u32x4*)(wvp + (ks) * 2048 + 1024)); \
                       bf16x16 b1_ = cat16u(*(const LAS u32x4*)(wvp + 16384 + (ks) * 2048), *(const LAS u32x4*)(wvp + 16384 + (ks) * 2048 + 1024)); \
                       PIN(b0_); PIN(b1_); NOP2(); ASMF_V(av0, af[ks], b0_, idx); ASMF_V(av1, af[ks], b1_, idx); } while (0)
        {
            float qs[4], ksum[4];
#pragma unroll
            for (int r = 0; r < 4; ++r) { qs[r] = swap32_add(aq0[r], aq1[r]) * CB; ksum[r] = swap32_add(ak0[r], ak1[r]); }
            *(LAS u32x2*)(qw) = (u32x2){cvtpk(qs[0], qs[1]), cvtpk(qs[2], qs[3])};
            *(LAS u32x2*)(kw) = (u32x2){cvtpk(ksum[0], ksum[1]), cvtpk(ksum[2], ksum[3])};
        }
        VSTEP(0); VSTEP(1);
        SBAR();
        LBAR();
        bf16x8 xf[4];
#pragma unroll
        for (int ks = 0; ks < 4; ++ks) xf[ks] = cat8(trread(xr + ks * 1024), trread(xr + ks * 1024 + 512));
        VSTEP(2); VSTEP(3);
        SBAR();
        VSTEP(4); VSTEP(5);
        SBAR();
        f32x4 sG, s0, s1;
        const LAS char* kxr = l16m + (RL_KX - RL_M) + kb * 8192;
#pragma unroll
        for (int ks = 0; ks < 4; ++ks) {
            bf16x8 k0 = asbf8(*(const LAS u32x4*)(kxr + ks * 1024)), k1 = asbf8(*(const LAS u32x4*)(kxr + 4096 + ks * 1024));
            PIN(xf[ks]); PIN(k0); PIN(k1); NOP2();
            if (ks == 0) { AMF0(sG, xf[ks], xf[ks]); AMF0(s0, k0, xf[ks]); AMF0(s1, k1, xf[ks]); }
            else { AMF(sG, xf[ks], xf[ks]); AMF(s0, k0, xf[ks]); AMF(s1, k1, xf[ks]); }
        }
        SBAR();
        VSTEP(6); VSTEP(7);
        FENCE3(sG, s0, s1); FENCE2(av0, av1);
        {
            bf16x8 xw = w == 0 ? xf[0] : (w == 1 ? xf[1] : (w == 2 ? xf[2] : xf[3]));
            __builtin_amdgcn_raw_buffer_store_b128(asu4(xw), rws, l16, OFF_XF + (s * 32 + h) * 4096 + w * 1024, 16);
        }
        if (g < 2) sG = (f32x4){-INFINITY, -INFINITY, -INFINITY, -INFINITY};
        float m = fmaxf(fmaxf(fmaxf(s0[0], s0[1]), fmaxf(s0[2], s0[3])), fmaxf(fmaxf(s1[0], s1[1]), fmaxf(s1[2], s1[3])));
        m = fmaxf(m, fmaxf(fmaxf(sG[0], sG[1]), fmaxf(sG[2], sG[3])));
        m = xmax16(m); m = xmax32(m);
        float p0[4], p1[4], pG[4], l = 0;
#pragma unroll
        for (int r = 0; r < 4; ++r) { p0[r] = __builtin_amdgcn_exp2f(s0[r] - m); p1[r] = __builtin_amdgcn_exp2f(s1[r] - m); pG[r] = __builtin_amdgcn_exp2f(sG[r] - m); l += p0[r] + p1[r] + pG[r]; }
        const u32x4 px = {cvtpk(p0[0], p0[1]), cvtpk(p0[2], p0[3]), cvtpk(p1[0], p1[1]), cvtpk(p1[2], p1[3])};
        const u32x2 pm = {cvtpk(pG[0], pG[1]), cvtpk(pG[2], pG[3])};
        u32x2 vm[2];
        vm[0] = (u32x2){cvtpk(xsum32(av0[0]), xsum32(av0[1])), cvtpk(xsum32(av0[2]), xsum32(av0[3]))};
        vm[1] = (u32x2){cvtpk(xsum32(av1[0]), xsum32(av1[1])), cvtpk(xsum32(av1[2]), xsum32(av1[3]))};
        __builtin_amdgcn_raw_buffer_store_b128((u32x4){vm[0][0], vm[0][1], vm[1][0], vm[1][1]}, rws, l16, OFF_VM + (s * 32 + h) * 4096 + w * 1024, 16);
        if (s == NSEG - 1) break;
        f32x4 o0, o1;
        { u32x4 px_ = px; u32x2 pm_ = pm; PIN(px_); PIN(pm_); PIN(vm[0]); PIN(vm[1]); PIN(vx[0]); PIN(vx[1]); NOP2();
          u32x4 vmm_ = {vm[0][0], vm[0][1], vm[1][0], vm[1][1]}, b0_ = {pm_[0], pm_[1], 0u, 0u}, b1_ = {0u, 0u, pm_[0], pm_[1]};
          PIN(vmm_); PIN(b0_); PIN(b1_); NOP2();
          AMF0(o0, vx[0], px_); AMF0(o1, vx[1], px_); AMF(o0, vmm_, b0_); AMF(o1, vmm_, b1_); FENCE2(o0, o1); }
        l = xsum16(l); l = xsum32(l);
        const float il = __builtin_amdgcn_rcpf(l);
        if (c < 8) {
            *(LAS u32x2*)(ow) = (u32x2){cvtpk(o0[0] * il, o0[1] * il), cvtpk(o0[2] * il, o0[3] * il)};
            *(LAS u32x2*)(ow + 32) = (u32x2){cvtpk(o1[0] * il, o1[1] * il), cvtpk(o1[2] * il, o1[3] * il)};
        }
        SBAR();
#pragma unroll
        for (int n = 0; n < 2; ++n) vx[n] = __builtin_amdgcn_raw_buffer_load_b128(rws, l16, vxs + (s + 1) * 262144 + n * 1024, 16);
        LBAR();
        bf16x8 of[2];
        of[0] = asbf8(*(const LAS u32x4*)(orr)); of[1] = asbf8(*(const LAS u32x4*)(orr + 128));
        const unsigned pslot = (s & 1) * 262144;
#pragma unroll
        for (int i = 0; i < 2; ++i) {
            f32x4 ac[4];
#pragma unroll
            for (int t = 0; t < 4; ++t) { ac[t] = (f32x4){0, 0, 0, 0}; PIN(ac[t]); }
            PIN(of[0]); PIN(of[1]); NOP2();
#pragma unroll
            for (int t = 0; t < 4; ++t) ASMF_V(ac[t], of[0], wo[4 * i + t][0], idx);
#pragma unroll
            for (int t = 0; t < 4; ++t) ASMF_V(ac[t], of[1], wo[4 * i + t][1], idx);
            FENCE4(ac[0], ac[1], ac[2], ac[3]);
            u32x4 v;
#pragma unroll
            for (int b = 0; b < 2; ++b) {
                float y[4];
#pragma unroll
                for (int r = 0; r < 4; ++r) y[r] = swap32_add(ac[2 * b][r], ac[2 * b + 1][r]);
                v[2 * b] = nosent(cvtpk(y[0], y[1])); v[2 * b + 1] = nosent(cvtpk(y[2], y[3]));
            }
            __builtin_amdgcn_raw_buffer_store_b128(v, rws, pst, OFF_P + w * 65536 + h * 64 + pslot + i * 32768, 16);
        }
        SBAR();
        u32x4 r0, r1;
        const unsigned pin = OFF_P + (h * 4 + w) * 2048 + pslot;
        for (unsigned sp = 0;;) {
            asm volatile("" ::: "memory");
            r0 = __builtin_amdgcn_raw_buffer_load_b128(rws, l16, pin, 16); r1 = __builtin_amdgcn_raw_buffer_load_b128(rws, l16, pin + 1024, 16);
            if (!__any(anysent(r0, r1))) break;
            if (++sp > SPIN_CAP || ((sp & 1023) == 0 && __hip_atomic_load(ctl, RLX_AGENT))) { __hip_atomic_store(ctl, 1u, RLX_AGENT); fail = true; break; }
        }
        f32x4 d;
        { bf16x8 sel_ = sel; PIN(sel_); PIN(r0); PIN(r1); NOP2(); AMF0(d, sel_, r0); AMF(d, sel_, r1); FENCE1(d); }
#pragma unroll
        for (int r = 0; r < 4; ++r) {
            d[r] += __uint_as_float(__builtin_amdgcn_update_dpp(0u, __float_as_uint(d[r]), 0x128, 0xf, 0xf, false));
            d[r] += __uint_as_float(__builtin_amdgcn_update_dpp(0u, __float_as_uint(d[r]), 0x124, 0xf, 0xf, false));
        }
        const unsigned mo = OFF_M + layer * 524288 + (s + 1) * 8192;
        if (c < 4 && g < 2) __builtin_amdgcn_raw_buffer_store_b64((u32x2){nosent(cvtpk(d[0], d[1])), nosent(cvtpk(d[2], d[3]))}, rws, mout, mo + (h * 4 + w) * 64, 16);
        for (unsigned sp = 0;;) {
            asm volatile("" ::: "memory");
            r0 = __builtin_amdgcn_raw_buffer_load_b128(rws, l16, mo + w * 2048, 16); r1 = __builtin_amdgcn_raw_buffer_load_b128(rws, l16, mo + w * 2048 + 1024, 16);
            if (!__any(anysent(r0, r1))) break;
            if (++sp > SPIN_CAP || ((sp & 1023) == 0 && __hip_atomic_load(ctl, RLX_AGENT))) { __hip_atomic_store(ctl, 1u, RLX_AGENT); fail = true; break; }
        }
        LAS char* mw = (LAS char*)l16m + w * 2048;
        *(LAS u32x4*)(mw) = r0; *(LAS u32x4*)(mw + 1024) = r1;
        {
            const u32x4 z = {~0u, ~0u, ~0u, ~0u};
            __builtin_amdgcn_raw_buffer_store_b128(z, rws, l16, pin, 16); __builtin_amdgcn_raw_buffer_store_b128(z, rws, l16, pin + 1024, 16);
        }
        LBAR();
        if (fail) break;
    }
}

__global__ void __launch_bounds__(256) k_attnB(Params p) {
    const int lane = threadIdx.x & 63, c = lane & 15, g = lane >> 4;
    const int task = blockIdx.x * 4 + (threadIdx.x >> 6), seg = task >> 5, h = task & 31;
    const char* kx = p.ws + OFF_KX + (size_t)task * 8192 + lane * 16;
    const char* qx = p.ws + OFF_QX + (size_t)task * 8192 + lane * 16;
    const char* vxp = p.ws + OFF_VX + (size_t)task * 8192 + lane * 16;
    const char* xfp = p.ws + OFF_XF + (size_t)task * 4096 + lane * 16;
    const char* vmp = p.ws + OFF_VM + (size_t)task * 4096 + lane * 16;
    f32x4 sc[2][3];
    for (int qt = 0; qt < 2; ++qt) for (int i = 0; i < 3; ++i) sc[qt][i] = (f32x4){0, 0, 0, 0};
#pragma unroll
    for (int ks = 0; ks < 4; ++ks) {
        bf16x8 km = asbf8(*(const u32x4*)(xfp + ks * 1024)), k0 = asbf8(*(const u32x4*)(kx + ks * 1024)), k1 = asbf8(*(const u32x4*)(kx + 4096 + ks * 1024));
#pragma unroll
        for (int qt = 0; qt < 2; ++qt) { bf16x8 q = asbf8(*(const u32x4*)(qx + qt * 4096 + ks * 1024));
            sc[qt][0] = MFMA16(km, q, sc[qt][0]); sc[qt][1] = MFMA16(k0, q, sc[qt][1]); sc[qt][2] = MFMA16(k1, q, sc[qt][2]); }
    }
    u32x4 px[2]; u32x2 pm[2]; float il[2];
#pragma unroll
    for (int qt = 0; qt < 2; ++qt) {
        const int qi = qt * 16 + c;
        float m = -INFINITY;
#pragma unroll
        for (int r = 0; r < 4; ++r) {
            if (g < 2) sc[qt][0][r] = -INFINITY;
            if (4 * g + r > qi) sc[qt][1][r] = -INFINITY;
            if (16 + 4 * g + r > qi) sc[qt][2][r] = -INFINITY;
            m = fmaxf(m, fmaxf(sc[qt][0][r], fmaxf(sc[qt][1][r], sc[qt][2][r])));
        }
        m = xmax16(m); m = xmax32(m);
        float l = 0, e[3][4];
#pragma unroll
        for (int i = 0; i < 3; ++i)
#pragma unroll
            for (int r = 0; r < 4; ++r) { e[i][r] = __builtin_amdgcn_exp2f(sc[qt][i][r] - m); l += e[i][r]; }
        l = xsum16(l); l = xsum32(l); il[qt] = 1.0f / l;
        pm[qt] = (u32x2){cvtpk(e[0][0], e[0][1]), cvtpk(e[0][2], e[0][3])};
        px[qt] = (u32x4){cvtpk(e[1][0], e[1][1]), cvtpk(e[1][2], e[1][3]), cvtpk(e[2][0], e[2][1]), cvtpk(e[2][2], e[2][3])};
    }
#pragma unroll
    for (int w = 0; w < 4; ++w) {
        u32x4 vmm = *(const u32x4*)(vmp + w * 1024);
#pragma unroll
        for (int t = 0; t < 2; ++t) {
            bf16x8 v = asbf8(*(const u32x4*)(vxp + (w * 2 + t) * 1024));
#pragma unroll
            for (int qt = 0; qt < 2; ++qt) {
                f32x4 o = {0, 0, 0, 0};
                const u32x4 pb = t == 0 ? (u32x4){pm[qt][0], pm[qt][1], 0u, 0u} : (u32x4){0u, 0u, pm[qt][0], pm[qt][1]};
                o = MFMA16(v, asbf8(px[qt]), o); o = MFMA16(asbf8(vmm), asbf8(pb), o);
                u16* op = (u16*)(p.ws + OFF_OB) + (size_t)(seg * 32 + qt * 16 + c) * 4096 + h * 128 + w * 32 + t * 16 + 4 * g;
                *(u32x2*)op = (u32x2){cvtpk(o[0] * il[qt], o[1] * il[qt]), cvtpk(o[2] * il[qt], o[3] * il[qt])};
            }
        }
    }
}

extern "C" void kernel_launch(void* const* d_in, const int* in_sizes, int n_in, void* d_out, int out_size, void* d_ws, size_t ws_size, hipStream_t stream) {
    Params p;
    p.x = (const u16*)d_in[0]; p.wqkv = (const u16*)d_in[1]; p.wo = (const u16*)d_in[2];
    p.mem0 = (const u16*)d_in[4]; p.Wq = (const u16*)d_in[5]; p.Wk = (const u16*)d_in[6]; p.Wv = (const u16*)d_in[7]; p.Wo = (const u16*)d_in[8];
    p.out = (u16*)d_out; p.ws = (char*)d_ws;
    hipFuncSetAttribute((const void*)k_attnA, hipFuncAttributeMaxDynamicSharedMemorySize, 131072 + 64);
    hipFuncSetAttribute((const void*)k_recur, hipFuncAttributeMaxDynamicSharedMemorySize, R_LDS);
    hipMemsetAsync(p.ws + OFF_CTL, 0, 4096, stream);
#define NLAYERS 2
    for (int layer = 0; layer < NLAYERS; ++layer) {
        if (layer == 0) k_ln<true><<<S / 4, 256, 0, stream>>>(p.x, (u16*)(p.ws + OFF_XN));
        else k_ln<false><<<S / 4, 256, 0, stream>>>(p.ws + OFF_XB, (u16*)(p.ws + OFF_XN));
        k_gemm<0><<<dim3(12, 16), 256, 0, stream>>>(p, layer);
        k_attnA<<<256, 256, 131072 + 64, stream>>>(p);
        k_gemm<1><<<dim3(4, 16), 256, 0, stream>>>(p, layer);
        k_ln<false><<<S / 4, 256, 0, stream>>>(p.ws + OFF_XA, (u16*)(p.ws + OFF_XN));
        k_gemm<2><<<dim3(96, 16), 256, 0, stream>>>(p, layer);
        k_recur<<<32, 256, R_LDS, stream>>>(p, layer);
        k_attnB<<<512, 256, 0, stream>>>(p);
        k_gemm<3><<<dim3(4, 16), 256, 0, stream>>>(p, layer);
    }
}
```

```cpp
#include <hip/hip_runtime.h>
#include <hip/hip_bf16.h>
#include <stdint.h>

typedef unsigned short u16;
typedef __bf16 bf16x8 __attribute__((ext_vector_type(8)));
typedef __bf16 bf16x2 __attribute__((ext_vector_type(2)));
typedef short s16x4 __attribute__((ext_vector_type(4)));
typedef short s16x8 __attribute__((ext_vector_type(8)));
typedef float f32x2 __attribute__((ext_vector_type(2)));
typedef float f32x4 __attribute__((ext_vector_type(4)));
typedef float f32x16 __attribute__((ext_vector_type(16)));
typedef unsigned u32x2 __attribute__((ext_vector_type(2)));
typedef unsigned u32x4 __attribute__((ext_vector_type(4)));
typedef unsigned u32x32 __attribute__((ext_vector_type(32)));
#define LAS __attribute__((address_space(3)))
#define GAS __attribute__((address_space(1)))
typedef GAS unsigned gu32;
#define RLX_AGENT __ATOMIC_RELAXED, __HIP_MEMORY_SCOPE_AGENT
#define DI __device__ __forceinline__
typedef __amdgpu_buffer_rsrc_t rsrc_t;

constexpr int NL = 2;
constexpr int S = 2048, D = 512, NSEG = 64;
constexpr unsigned MB = 1u << 20;
constexpr unsigned OFF_CTL = 0;
constexpr unsigned OFF_PM = 2 * MB;
constexpr unsigned OFF_PM1 = 4 * MB;
constexpr unsigned OFF_XA32 = 6 * MB;
constexpr unsigned OFF_XA16 = 14 * MB;
constexpr unsigned OFF_XB32 = 18 * MB;
constexpr unsigned OFF_XB16 = 22 * MB;
constexpr unsigned OFF_QA = 24 * MB;
constexpr unsigned OFF_KA = 28 * MB;
constexpr unsigned OFF_VA = 32 * MB;
constexpr unsigned OFF_OA = 36 * MB;
constexpr unsigned OFF_KX = 40 * MB;
constexpr unsigned OFF_VX = 72 * MB;
constexpr unsigned OFF_XF = 104 * MB;
constexpr unsigned OFF_VM = 120 * MB;
constexpr unsigned OFF_PB = 128 * MB;
constexpr unsigned OFF_ST = 192 * MB;
constexpr unsigned OFF_DBG = 200 * MB;
constexpr unsigned WS_SPAN = 0x0d000000;
constexpr int C_TMO = 0, C_WCNT = 16, C_XCNT = 32  , C_RBAR = C_XCNT + 256, C_PAR = C_RBAR + 16  ,
              C_T1P = C_PAR + 32  , C_PA = C_T1P + 192  , C_PG = C_PA + 128  ,
              C_PQ = C_PG + 64  , C_PR = C_PQ + 128  , C_FKV = C_PR + 32  ,
              C_FR = C_FKV + 64 * 16  , C_KN = C_FR + 64 * 16  , C_END = C_KN + 1024;
constexpr unsigned CTL_BYTES = 16384;
static_assert(C_END * 4 <= CTL_BYTES, "ctl");

constexpr int RED_SPLIT = 49;
__device__ __forceinline__ bool red_by_g(int s) { return s >= RED_SPLIT && (s & 1); }
constexpr float LOG2E = 1.4426950408889634f;
constexpr float CA = 0.25f * LOG2E;
constexpr float CB = 0.08838834764831845f * LOG2E;
constexpr int LDS_BYTES = 163840;
constexpr unsigned SPIN_CAP = 1u << 18;

DI unsigned cvtpk(float lo, float hi) { f32x2 v = {lo, hi}; return __builtin_bit_cast(unsigned, __builtin_convertvector(v, bf16x2)); }
DI float bflo(unsigned v) { return __uint_as_float(v << 16); }
DI float bfhi(unsigned v) { return __uint_as_float(v & 0xffff0000u); }
DI s16x4 trread(const LAS char* p) { return __builtin_amdgcn_ds_read_tr16_b64_v4i16((LAS s16x4*)p); }
DI bf16x8 cat8(s16x4 a, s16x4 b) { s16x8 r = {a[0], a[1], a[2], a[3], b[0], b[1], b[2], b[3]}; return __builtin_bit_cast(bf16x8, r); }
DI bf16x8 asbf8(u32x4 v) { return __builtin_bit_cast(bf16x8, v); }
DI u32x4 asu4(bf16x8 v) { return __builtin_bit_cast(u32x4, v); }
DI u32x4 ldsr(const LAS char* p) { return *(const LAS u32x4*)p; }
DI void ldsw(LAS char* p, u32x4 v) { *(LAS u32x4*)p = v; }
#define MFMA16(a, b, c) __builtin_amdgcn_mfma_f32_16x16x32_bf16(a, b, c, 0, 0, 0)
#define MFMA32(a, b, c) __builtin_amdgcn_mfma_f32_32x32x16_bf16(a, b, c, 0, 0, 0)
#define LBAR() asm volatile("s_waitcnt lgkmcnt(0)\n\ts_barrier" ::: "memory")
#define SBAR() __builtin_amdgcn_sched_barrier(0)
DI float xmax32(float a) { auto r = __builtin_amdgcn_permlane32_swap(__float_as_uint(a), __float_as_uint(a), false, false); return fmaxf(__uint_as_float(r[0]), __uint_as_float(r[1])); }
DI float xmax16(float a) { auto r = __builtin_amdgcn_permlane16_swap(__float_as_uint(a), __float_as_uint(a), false, false); return fmaxf(__uint_as_float(r[0]), __uint_as_float(r[1])); }
DI float xsum32(float a) { auto r = __builtin_amdgcn_permlane32_swap(__float_as_uint(a), __float_as_uint(a), false, false); return __uint_as_float(r[0]) + __uint_as_float(r[1]); }
DI float xsum16(float a) { auto r = __builtin_amdgcn_permlane16_swap(__float_as_uint(a), __float_as_uint(a), false, false); return __uint_as_float(r[0]) + __uint_as_float(r[1]); }
DI u32x4 gld16(rsrc_t r, unsigned voff, unsigned soff) { return __builtin_amdgcn_raw_buffer_load_b128(r, voff, soff, 16); }
DI u32x2 gld8(rsrc_t r, unsigned voff, unsigned soff) { return __builtin_amdgcn_raw_buffer_load_b64(r, voff, soff, 16); }
DI void gst16(u32x4 v, rsrc_t r, unsigned voff, unsigned soff) { __builtin_amdgcn_raw_buffer_store_b128(v, r, voff, soff, 16); asm volatile("s_nop 1" :: "v"(v)); }
DI void gst8(u32x2 v, rsrc_t r, unsigned voff, unsigned soff) { __builtin_amdgcn_raw_buffer_store_b64(v, r, voff, soff, 16); }
DI void gst4(unsigned v, rsrc_t r, unsigned voff, unsigned soff) { __builtin_amdgcn_raw_buffer_store_b32(v, r, voff, soff, 16); }
DI void dma16(rsrc_t r, unsigned voff, unsigned soff, unsigned ldsbase) {
    unsigned sv;
    soff = __builtin_amdgcn_readfirstlane(soff); ldsbase = __builtin_amdgcn_readfirstlane(ldsbase);
    asm volatile("s_mov_b32 %0, m0\n\ts_mov_b32 m0, %4\n\ts_nop 3\n\tbuffer_load_dwordx4 %1, %2, %3 offen sc1 lds\n\ts_mov_b32 m0, %0" : "=&s"(sv) : "v"(voff), "s"(r), "s"(soff), "s"(ldsbase) : "memory");
}

struct Params {
    const u16 *x, *wqkv, *wo, *mem0, *Wq, *Wk, *Wv, *Wo;
    u16* out;
    char* ws;
    int d1, d2, dd, de;
};

DI void publish(gu32* ctl, int idx, unsigned v, bool leader) {
    asm volatile("s_waitcnt vmcnt(0)" ::: "memory");
    __syncthreads();
    if (leader) __hip_atomic_store(ctl + idx, v, RLX_AGENT);
}
DI void publish(gu32* ctl, int idx, unsigned v) { publish(ctl, idx, v, threadIdx.x == 0); }
DI unsigned peek_words(gu32* ctl, int idx, int n, int lane) {
    unsigned v = ~0u;
    if (lane < n) v = __hip_atomic_load(ctl + idx + lane, RLX_AGENT);
    v = min(v, (unsigned)__builtin_amdgcn_update_dpp(~0u, v, 0x128, 0xf, 0xf, false)); v = min(v, (unsigned)__builtin_amdgcn_update_dpp(~0u, v, 0x124, 0xf, 0xf, false));
    v = min(v, (unsigned)__builtin_amdgcn_update_dpp(~0u, v, 0x122, 0xf, 0xf, false)); v = min(v, (unsigned)__builtin_amdgcn_update_dpp(~0u, v, 0x121, 0xf, 0xf, false));
    { auto r = __builtin_amdgcn_permlane16_swap(v, v, false, false); v = min(r[0], r[1]); r = __builtin_amdgcn_permlane32_swap(v, v, false, false); v = min(r[0], r[1]); }
    return __builtin_amdgcn_readfirstlane(v);
}
DI void wait_words(gu32* ctl, int idx, int n, unsigned need, unsigned& known, int lane, int nap = 2) {
    if (known >= need) return;
    unsigned sp = 0;
    for (;;) {
        known = peek_words(ctl, idx, n, lane);
        if (known >= need) break;
        for (int i = 0; i < nap; ++i) __builtin_amdgcn_s_sleep(1);
        if (++sp > SPIN_CAP || ((sp & 63u) == 0 && __builtin_amdgcn_readfirstlane(__hip_atomic_load(ctl + C_TMO, RLX_AGENT)))) { __hip_atomic_store(ctl + C_TMO, 1u, RLX_AGENT); known = ~0u; break; }
        asm volatile("" ::: "memory");
    }
    asm volatile("" ::: "memory");
}
DI void wait_words_wg(gu32* ctl, int idx, int n, unsigned need, unsigned& known, int lane, int w, LAS unsigned* slot, int nap) {
    if (known >= need) return;
    if (w == 0) { wait_words(ctl, idx, n, need, known, lane, nap); if (lane == 0) *slot = known; }
    asm volatile("s_waitcnt lgkmcnt(0)\n\ts_barrier" ::: "memory");
    known = __builtin_amdgcn_readfirstlane(*slot);
    asm volatile("s_waitcnt lgkmcnt(0)\n\ts_barrier" ::: "memory");
}
DI int fresh_lane() { int l; asm volatile("v_mbcnt_lo_u32_b32 %0, -1, 0\n\tv_mbcnt_hi_u32_b32 %0, -1, %0" : "=v"(l)); return l; }
DI void signal_set(gu32* ctl, int idx, unsigned v) {
    asm volatile("s_waitcnt vmcnt(0)" ::: "memory");
    __syncthreads();
    if (threadIdx.x == 0) __hip_atomic_store(ctl + idx, v, RLX_AGENT);
}

#define D1 14
#define D2 20
#define DD 8
#define DE 8
#define R_ASM \
  "v_mbcnt_lo_u32_b32 v15, -1, 0\n" \
  "v_mbcnt_hi_u32_b32 v15, -1, v15\n" \
  "v_lshlrev_b32 v0, 4, v15\n" \
  "v_and_b32 v16, 15, v15\n" \
  "v_lshrrev_b32 v17, 4, v15\n" \
  "v_lshrrev_b32 v18, 2, v16\n" \
  "v_and_b32 v19, 3, v16\n" \
  "v_mov_b32 v20, 0x8888\n" \
  "v_mov_b32 v21, 0xdddd\n" \
  "v_cmp_gt_u32 vcc, 8, v16\n" \
  "v_cndmask_b32 v1, v21, v20, vcc\n" \
  "v_lshrrev_b32 v20, 1, v19\n" \
  "v_lshlrev_b32 v20, 3, v20\n" \
  "v_lshl_add_u32 v20, v18, 4, v20\n" \
  "v_and_b32 v21, 1, v17\n" \
  "v_lshl_add_u32 v20, v21, 6, v20\n" \
  "v_and_b32 v22, 1, v19\n" \
  "v_lshl_add_u32 v20, v22, 7, v20\n" \
  "v_lshrrev_b32 v22, 1, v17\n" \
  "v_lshl_add_u32 v20, v22, 8, v20\n" \
  "s_add_u32 s73, s78, 131072\n" \
  "v_add_u32 v2, s73, v20\n" \
  "s_lshl_b32 s73, s77, 15\n" \
  "s_add_u32 s73, s73, s78\n" \
  "v_add_u32 v3, s73, v0\n" \
  "s_lshl_b32 s73, s77, 5\n" \
  "v_lshrrev_b32 v22, 1, v17\n" \
  "v_lshl_add_u32 v20, v22, 4, v16\n" \
  "v_add_u32 v20, s73, v20\n" \
  "v_lshlrev_b32 v20, 5, v20\n" \
  "s_add_u32 s73, s78, 139264\n" \
  "v_add_u32 v20, s73, v20\n" \
  "v_xor_b32 v22, v21, v18\n" \
  "v_lshl_add_u32 v4, v22, 3, v20\n" \
  "v_xor_b32 v22, 2, v22\n" \
  "v_lshl_add_u32 v5, v22, 3, v20\n" \
  "v_lshl_add_u32 v20, v17, 2, v18\n" \
  "v_lshlrev_b32 v20, 5, v20\n" \
  "v_add_u32 v20, s73, v20\n" \
  "v_xor_b32 v22, v19, v17\n" \
  "v_lshl_add_u32 v6, v22, 3, v20\n" \
  "v_and_b32 v22, 1, v19\n" \
  "v_or_b32 v22, 2, v22\n" \
  "v_xor_b32 v22, v22, v17\n" \
  "v_lshl_add_u32 v7, v22, 3, v20\n" \
  "s_lshl_b32 s73, s77, 11\n" \
  "s_add_u32 s74, s73, s78\n" \
  "s_add_u32 s70, s74, 145920\n" \
  "s_add_u32 s71, s74, 154112\n" \
  "s_add_u32 s74, s74, 131072\n" \
  "v_add_u32 v8, s74, v0\n" \
  "s_add_u32 s74, s78, 145920\n" \
  "v_add_u32 v13, s74, v0\n" \
  "v_and_b32 v20, 7, v16\n" \
  "v_mul_u32_u24 v20, 272, v20\n" \
  "s_lshl_b32 s74, s77, 6\n" \
  "s_add_u32 s74, s74, s78\n" \
  "s_add_u32 s74, s74, 143360\n" \
  "v_lshl_add_u32 v22, v17, 3, v20\n" \
  "v_add_u32 v22, s74, v22\n" \
  "v_and_b32 v21, 7, v15\n" \
  "v_lshlrev_b32 v21, 3, v21\n" \
  "s_add_u32 s74, s78, 145664\n" \
  "v_add_u32 v21, s74, v21\n" \
  "v_cmp_gt_u32 vcc, 8, v16\n" \
  "v_cndmask_b32 v9, v21, v22, vcc\n" \
  "v_lshrrev_b32 v21, 3, v16\n" \
  "v_lshlrev_b32 v21, 4, v21\n" \
  "v_lshl_add_u32 v21, v17, 5, v21\n" \
  "v_add_u32 v20, v20, v21\n" \
  "s_add_u32 s74, s78, 143360\n" \
  "v_add_u32 v10, s74, v20\n" \
  "v_lshrrev_b32 v20, 1, v17\n" \
  "v_lshrrev_b32 v21, 3, v16\n" \
  "v_lshl_add_u32 v20, v20, 1, v21\n" \
  "v_lshlrev_b32 v20, 14, v20\n" \
  "v_and_b32 v21, 1, v17\n" \
  "v_and_b32 v22, 1, v18\n" \
  "v_lshl_add_u32 v21, v21, 1, v22\n" \
  "v_lshl_add_u32 v20, v21, 11, v20\n" \
  "v_lshl_add_u32 v11, v19, 4, v20\n" \
  "v_and_b32 v21, 1, v17\n" \
  "v_lshlrev_b32 v21, 3, v21\n" \
  "v_lshl_add_u32 v12, v19, 4, v21\n" \
  "v_lshrrev_b32 v20, 1, v16\n" \
  "v_cmp_eq_u32 s[62:63], 0, v20\n" \
  "v_cmp_eq_u32 s[64:65], 1, v20\n" \
  "v_cmp_eq_u32 s[66:67], 2, v20\n" \
  "v_cmp_eq_u32 s[68:69], 3, v20\n" \
  "s_nop 1\n" \
  "s_mov_b32 s52, 0x3e0293ee\n" \
  "s_mov_b32 s53, s52\n" \
  "s_mov_b32 s54, 0x000f000f\n" \
  "s_mov_b32 s55, 0\n" \
  "s_mov_b32 s56, 0x00ff00ff\n" \
  "s_mov_b32 s57, 0x00ff00ff\n" \
  "s_mov_b32 s58, 0\n" \
  "s_mov_b32 s59, -1\n" \
  "s_mov_b32 s60, -1\n" \
  "s_mov_b32 s61, 0\n" \
  "s_mov_b32 s44, 0\n" \
  "s_lshl_b32 s73, s77, 17\n" \
  "s_add_u32 s45, s79, s73\n" \
  "s_lshl_b32 s73, s76, 6\n" \
  "s_add_u32 s45, s45, s73\n" \
  "s_lshl_b32 s73, s76, 14\n" \
  "s_add_u32 s46, s79, s73\n" \
  "s_lshl_b32 s73, s77, 11\n" \
  "s_add_u32 s46, s46, s73\n" \
  "s_add_u32 s47, s80, 16384\n" \
  "s_lshl_b32 s73, s76, 13\n" \
  "s_lshl_b32 s74, s77, 11\n" \
  "s_add_u32 s73, s73, s74\n" \
  "s_add_u32 s73, s73, 262144\n" \
  "s_add_u32 s48, s81, s73\n" \
  "s_add_u32 s49, s82, s73\n" \
  "s_lshl_b32 s73, s76, 12\n" \
  "s_lshl_b32 s74, s77, 10\n" \
  "s_add_u32 s73, s73, s74\n" \
  "s_add_u32 s50, s83, s73\n" \
  "s_lshl_b32 s73, s76, 11\n" \
  "s_lshl_b32 s74, s77, 9\n" \
  "s_add_u32 s73, s73, s74\n" \
  "s_add_u32 s51, s84, s73\n" \
  "s_mov_b32 s36, 0\n" \
  "s_mov_b32 s37, 0\n" \
  "s_sub_u32 s34, s82, s81\n" \
  "ds_read_b128 v[64:67], v3\n" \
  "ds_read_b128 v[68:71], v3 offset:1024\n" \
  "ds_read_b128 v[72:75], v3 offset:2048\n" \
  "ds_read_b128 v[76:79], v3 offset:3072\n" \
  "ds_read_b128 v[80:83], v3 offset:4096\n" \
  "ds_read_b128 v[84:87], v3 offset:5120\n" \
  "ds_read_b128 v[88:91], v3 offset:6144\n" \
  "ds_read_b128 v[92:95], v3 offset:7168\n" \
  "ds_read_b128 v[96:99], v3 offset:8192\n" \
  "ds_read_b128 v[100:103], v3 offset:9216\n" \
  "ds_read_b128 v[104:107], v3 offset:10240\n" \
  "ds_read_b128 v[108:111], v3 offset:11264\n" \
  "ds_read_b128 v[112:115], v3 offset:12288\n" \
  "ds_read_b128 v[116:119], v3 offset:13312\n" \
  "ds_read_b128 v[120:123], v3 offset:14336\n" \
  "ds_read_b128 v[124:127], v3 offset:15360\n" \
  "v_mov_b64 v[48:49], 0\n" \
  "v_mov_b64 v[50:51], 0\n" \
  "v_mov_b64 v[52:53], 0\n" \
  "v_mov_b64 v[54:55], 0\n" \
  "v_mov_b64 v[56:57], 0\n" \
  "v_mov_b64 v[58:59], 0\n" \
  "v_mov_b64 v[60:61], 0\n" \
  "v_mov_b64 v[62:63], 0\n" \
  "s_waitcnt lgkmcnt(0)\n" \
  "s_nop 7\n" \
  "RL_LOOP_%=:\n" \
  "ds_read_b64_tr_b16 v[16:17], v2\n" \
  "ds_read_b64_tr_b16 v[18:19], v2 offset:512\n" \
  "ds_read_b64_tr_b16 v[20:21], v2 offset:1024\n" \
  "ds_read_b64_tr_b16 v[22:23], v2 offset:1536\n" \
  "ds_read_b64_tr_b16 v[24:25], v2 offset:2048\n" \
  "ds_read_b64_tr_b16 v[26:27], v2 offset:2560\n" \
  "ds_read_b64_tr_b16 v[28:29], v2 offset:3072\n" \
  "ds_read_b64_tr_b16 v[30:31], v2 offset:3584\n" \
  "s_waitcnt lgkmcnt(6)\n" \
  "v_smfmac_f32_16x16x64_bf16 v[56:59], v[16:19], v[64:71], v1\n" \
  "ds_read_b64_tr_b16 v[32:33], v2 offset:4096\n" \
  "ds_read_b64_tr_b16 v[34:35], v2 offset:4608\n" \
  "ds_read_b64_tr_b16 v[36:37], v2 offset:5120\n" \
  "ds_read_b64_tr_b16 v[38:39], v2 offset:5632\n" \
  "v_smfmac_f32_16x16x64_bf16 v[60:63], v[16:19], v[72:79], v1\n" \
  "ds_read_b64_tr_b16 v[40:41], v2 offset:6144\n" \
  "ds_read_b64_tr_b16 v[42:43], v2 offset:6656\n" \
  "ds_read_b64_tr_b16 v[44:45], v2 offset:7168\n" \
  "ds_read_b64_tr_b16 v[46:47], v2 offset:7680\n" \
  "s_waitcnt lgkmcnt(12)\n" \
  "v_smfmac_f32_16x16x64_bf16 v[56:59], v[20:23], v[80:87], v1\n" \
  "v_mov_b64 v[64:65], 0\n" \
  "v_smfmac_f32_16x16x64_bf16 v[60:63], v[20:23], v[88:95], v1\n" \
  "v_mov_b64 v[66:67], 0\n" \
  "s_waitcnt lgkmcnt(10)\n" \
  "v_smfmac_f32_16x16x64_bf16 v[56:59], v[24:27], v[96:103], v1\n" \
  "v_mov_b64 v[68:69], 0\n" \
  "v_smfmac_f32_16x16x64_bf16 v[60:63], v[24:27], v[104:111], v1\n" \
  "v_mov_b64 v[70:71], 0\n" \
  "s_waitcnt lgkmcnt(8)\n" \
  "v_smfmac_f32_16x16x64_bf16 v[56:59], v[28:31], v[112:119], v1\n" \
  "ds_read_b128 v[72:75], v3 offset:16384\n" \
  "v_smfmac_f32_16x16x64_bf16 v[60:63], v[28:31], v[120:127], v1\n" \
  "ds_read_b128 v[76:79], v3 offset:17408\n" \
  "s_waitcnt lgkmcnt(8)\n" \
  "v_smfmac_f32_16x16x64_bf16 v[56:59], v[32:35], a[128:135], v1\n" \
  "ds_read_b128 v[80:83], v3 offset:18432\n" \
  "v_smfmac_f32_16x16x64_bf16 v[60:63], v[32:35], a[136:143], v1\n" \
  "ds_read_b128 v[84:87], v3 offset:19456\n" \
  "s_waitcnt lgkmcnt(8)\n" \
  "v_smfmac_f32_16x16x64_bf16 v[56:59], v[36:39], a[144:151], v1\n" \
  "ds_read_b128 v[88:91], v3 offset:20480\n" \
  "v_smfmac_f32_16x16x64_bf16 v[60:63], v[36:39], a[152:159], v1\n" \
  "ds_read_b128 v[92:95], v3 offset:21504\n" \
  "s_waitcnt lgkmcnt(8)\n" \
  "v_smfmac_f32_16x16x64_bf16 v[56:59], v[40:43], a[160:167], v1\n" \
  "ds_read_b128 v[96:99], v3 offset:22528\n" \
  "v_smfmac_f32_16x16x64_bf16 v[60:63], v[40:43], a[168:175], v1\n" \
  "ds_read_b128 v[100:103], v3 offset:23552\n" \
  "s_waitcnt lgkmcnt(8)\n" \
  "v_smfmac_f32_16x16x64_bf16 v[56:59], v[44:47], a[176:183], v1\n" \
  "ds_read_b128 v[104:107], v3 offset:24576\n" \
  "v_smfmac_f32_16x16x64_bf16 v[60:63], v[44:47], a[184:191], v1\n" \
  "ds_read_b128 v[108:111], v3 offset:25600\n" \
  "v_smfmac_f32_16x16x64_bf16 v[48:51], v[16:19], a[0:7], v1\n" \
  "ds_read_b128 v[112:115], v3 offset:26624\n" \
  "v_smfmac_f32_16x16x64_bf16 v[52:55], v[16:19], a[8:15], v1\n" \
  "ds_read_b128 v[116:119], v3 offset:27648\n" \
  "v_smfmac_f32_16x16x64_bf16 v[48:51], v[20:23], a[16:23], v1\n" \
  "ds_read_b128 v[120:123], v3 offset:28672\n" \
  "v_smfmac_f32_16x16x64_bf16 v[52:55], v[20:23], a[24:31], v1\n" \
  "ds_read_b128 v[124:127], v3 offset:29696\n" \
  "v_smfmac_f32_16x16x64_bf16 v[48:51], v[24:27], a[32:39], v1\n" \
  "v_permlane32_swap_b32 v56, v60\n" \
  "v_smfmac_f32_16x16x64_bf16 v[52:55], v[24:27], a[40:47], v1\n" \
  "v_permlane32_swap_b32 v57, v61\n" \
  "v_smfmac_f32_16x16x64_bf16 v[48:51], v[28:31], a[48:55], v1\n" \
  "v_permlane32_swap_b32 v58, v62\n" \
  "v_smfmac_f32_16x16x64_bf16 v[52:55], v[28:31], a[56:63], v1\n" \
  "v_permlane32_swap_b32 v59, v63\n" \
  "v_smfmac_f32_16x16x64_bf16 v[48:51], v[32:35], a[64:71], v1\n" \
  "v_pk_add_f32 v[56:57], v[56:57], v[60:61]\n" \
  "v_smfmac_f32_16x16x64_bf16 v[52:55], v[32:35], a[72:79], v1\n" \
  "v_pk_add_f32 v[58:59], v[58:59], v[62:63]\n" \
  "v_smfmac_f32_16x16x64_bf16 v[48:51], v[36:39], a[80:87], v1\n" \
  "v_pk_mul_f32 v[56:57], v[56:57], s[52:53]\n" \
  "v_smfmac_f32_16x16x64_bf16 v[52:55], v[36:39], a[88:95], v1\n" \
  "v_pk_mul_f32 v[58:59], v[58:59], s[52:53]\n" \
  "v_smfmac_f32_16x16x64_bf16 v[48:51], v[40:43], a[96:103], v1\n" \
  "v_cvt_pk_bf16_f32 v56, v56, v57\n" \
  "v_smfmac_f32_16x16x64_bf16 v[52:55], v[40:43], a[104:111], v1\n" \
  "v_cvt_pk_bf16_f32 v57, v58, v59\n" \
  "v_smfmac_f32_16x16x64_bf16 v[48:51], v[44:47], a[112:119], v1\n" \
  "ds_write_b64 v5, v[56:57]\n" \
  "v_smfmac_f32_16x16x64_bf16 v[52:55], v[44:47], a[120:127], v1\n" \
  "s_waitcnt lgkmcnt(13)\n" \
  "v_smfmac_f32_16x16x64_bf16 v[64:67], v[32:35], v[72:79], v1\n" \
  "ds_read_b128 v[72:75], v3 offset:30720\n" \
  "ds_read_b128 v[76:79], v3 offset:31744\n" \
  "s_waitcnt lgkmcnt(13)\n" \
  "v_smfmac_f32_16x16x64_bf16 v[68:71], v[32:35], v[80:87], v1\n" \
  "s_waitcnt lgkmcnt(11)\n" \
  "v_smfmac_f32_16x16x64_bf16 v[64:67], v[36:39], v[88:95], v1\n" \
  "v_permlane32_swap_b32 v48, v52\n" \
  "v_permlane32_swap_b32 v49, v53\n" \
  "v_permlane32_swap_b32 v50, v54\n" \
  "v_permlane32_swap_b32 v51, v55\n" \
  "v_pk_add_f32 v[48:49], v[48:49], v[52:53]\n" \
  "v_pk_add_f32 v[50:51], v[50:51], v[54:55]\n" \
  "v_cvt_pk_bf16_f32 v48, v48, v49\n" \
  "v_cvt_pk_bf16_f32 v49, v50, v51\n" \
  "ds_write_b64 v4, v[48:49]\n" \
  "s_waitcnt lgkmcnt(10)\n" \
  "v_smfmac_f32_16x16x64_bf16 v[68:71], v[36:39], v[96:103], v1\n" \
  "ds_read_b128 v[80:83], v13\n" \
  "ds_read_b128 v[84:87], v13 offset:4096\n" \
  "ds_read_b128 v[88:91], v13 offset:1024\n" \
  "ds_read_b128 v[92:95], v13 offset:5120\n" \
  "s_waitcnt lgkmcnt(12)\n" \
  "v_smfmac_f32_16x16x64_bf16 v[64:67], v[40:43], v[104:111], v1\n" \
  "s_waitcnt lgkmcnt(10)\n" \
  "v_smfmac_f32_16x16x64_bf16 v[68:71], v[40:43], v[112:119], v1\n" \
  "s_waitcnt lgkmcnt(8)\n" \
  "v_smfmac_f32_16x16x64_bf16 v[64:67], v[44:47], v[120:127], v1\n" \
  "ds_read_b128 v[96:99], v13 offset:2048\n" \
  "ds_read_b128 v[100:103], v13 offset:6144\n" \
  "s_waitcnt lgkmcnt(6)\n" \
  "s_barrier\n" \
  "ds_read_b64_tr_b16 v[48:49], v6\n" \
  "ds_read_b64_tr_b16 v[50:51], v6 offset:512\n" \
  "ds_read_b64_tr_b16 v[52:53], v6 offset:1024\n" \
  "ds_read_b64_tr_b16 v[54:55], v6 offset:1536\n" \
  "ds_read_b64_tr_b16 v[56:57], v6 offset:2048\n" \
  "ds_read_b64_tr_b16 v[58:59], v6 offset:2560\n" \
  "ds_read_b64_tr_b16 v[60:61], v6 offset:3072\n" \
  "ds_read_b64_tr_b16 v[62:63], v6 offset:3584\n" \
  "v_smfmac_f32_16x16x64_bf16 v[68:71], v[44:47], v[72:79], v1\n" \
  "ds_read_b64_tr_b16 v[32:33], v7\n" \
  "ds_read_b64_tr_b16 v[34:35], v7 offset:512\n" \
  "v_smfmac_f32_16x16x64_bf16 v[64:67], v[16:19], a[192:199], v1\n" \
  "ds_read_b128 v[72:75], v13 offset:3072\n" \
  "v_smfmac_f32_16x16x64_bf16 v[68:71], v[16:19], a[200:207], v1\n" \
  "ds_read_b128 v[76:79], v13 offset:7168\n" \
  "v_smfmac_f32_16x16x64_bf16 v[64:67], v[20:23], a[208:215], v1\n" \
  "ds_read_b64_tr_b16 v[36:37], v7 offset:1024\n" \
  "ds_read_b64_tr_b16 v[38:39], v7 offset:1536\n" \
  "s_waitcnt lgkmcnt(12)\n" \
  "v_mfma_f32_16x16x32_bf16 v[108:111], v[80:83], v[48:51], 0\n" \
  "ds_read_b64_tr_b16 v[40:41], v7 offset:2048\n" \
  "v_mfma_f32_16x16x32_bf16 v[112:115], v[84:87], v[48:51], 0\n" \
  "ds_read_b64_tr_b16 v[42:43], v7 offset:2560\n" \
  "s_waitcnt lgkmcnt(6)\n" \
  "v_mfma_f32_16x16x32_bf16 v[104:107], v[32:35], v[48:51], 0\n" \
  "v_mfma_f32_16x16x32_bf16 v[108:111], v[88:91], v[52:55], v[108:111]\n" \
  "ds_read_b64_tr_b16 v[44:45], v7 offset:3072\n" \
  "v_mfma_f32_16x16x32_bf16 v[112:115], v[92:95], v[52:55], v[112:115]\n" \
  "ds_read_b64_tr_b16 v[46:47], v7 offset:3584\n" \
  "s_waitcnt lgkmcnt(4)\n" \
  "v_mfma_f32_16x16x32_bf16 v[104:107], v[36:39], v[52:55], v[104:107]\n" \
  "v_mfma_f32_16x16x32_bf16 v[108:111], v[96:99], v[56:59], v[108:111]\n" \
  "ds_read_b128 v[120:123], v8 offset:23040\n" \
  "v_mfma_f32_16x16x32_bf16 v[112:115], v[100:103], v[56:59], v[112:115]\n" \
  "ds_read_b128 v[124:127], v8 offset:24064\n" \
  "s_waitcnt lgkmcnt(4)\n" \
  "v_mfma_f32_16x16x32_bf16 v[104:107], v[40:43], v[56:59], v[104:107]\n" \
  "v_mfma_f32_16x16x32_bf16 v[108:111], v[72:75], v[60:63], v[108:111]\n" \
  "v_mfma_f32_16x16x32_bf16 v[112:115], v[76:79], v[60:63], v[112:115]\n" \
  "s_waitcnt lgkmcnt(2)\n" \
  "v_mfma_f32_16x16x32_bf16 v[104:107], v[44:47], v[60:63], v[104:107]\n" \
  "v_smfmac_f32_16x16x64_bf16 v[68:71], v[20:23], a[216:223], v1\n" \
  "v_mov_b32 v116, 0x3f803f80\n" \
  "v_smfmac_f32_16x16x64_bf16 v[64:67], v[24:27], a[224:231], v1\n" \
  "v_mov_b32 v117, 0x3f803f80\n" \
  "v_smfmac_f32_16x16x64_bf16 v[68:71], v[24:27], a[232:239], v1\n" \
  "v_mov_b32 v118, 0x3f803f80\n" \
  "v_smfmac_f32_16x16x64_bf16 v[64:67], v[28:31], a[240:247], v1\n" \
  "v_mov_b32 v119, 0x3f803f80\n" \
  "v_smfmac_f32_16x16x64_bf16 v[68:71], v[28:31], a[248:255], v1\n" \
  "s_mov_b32 s75, 0\n" \
  "s_cmp_eq_u32 s44, 63\n" \
  "s_cbranch_scc1 RL_LAST_%=\n" \
  "v_exp_f32 v80, v108\n" \
  "v_exp_f32 v81, v109\n" \
  "v_exp_f32 v82, v110\n" \
  "v_exp_f32 v83, v111\n" \
  "v_exp_f32 v84, v112\n" \
  "v_exp_f32 v85, v113\n" \
  "v_exp_f32 v86, v114\n" \
  "v_exp_f32 v87, v115\n" \
  "v_cvt_pk_bf16_f32 v92, v80, v81\n" \
  "v_cvt_pk_bf16_f32 v93, v82, v83\n" \
  "v_cvt_pk_bf16_f32 v94, v84, v85\n" \
  "v_cvt_pk_bf16_f32 v95, v86, v87\n" \
  "v_exp_f32 v88, v104\n" \
  "v_exp_f32 v89, v105\n" \
  "s_waitcnt lgkmcnt(1)\n" \
  "v_mfma_f32_16x16x32_bf16 v[16:19], v[120:123], v[92:95], 0\n" \
  "v_exp_f32 v90, v106\n" \
  "s_waitcnt lgkmcnt(0)\n" \
  "v_mfma_f32_16x16x32_bf16 v[20:23], v[124:127], v[92:95], 0\n" \
  "v_exp_f32 v91, v107\n" \
  "v_mfma_f32_16x16x32_bf16 v[40:43], v[116:119], v[92:95], 0\n" \
  "v_mov_b64 v[26:27], 0\n" \
  "v_mov_b64 v[30:31], 0\n" \
  "v_cvt_pk_bf16_f32 v96, v88, v89\n" \
  "v_cvt_pk_bf16_f32 v97, v90, v91\n" \
  "v_cndmask_b32_e64 v28, 0, v96, s[58:59]\n" \
  "v_cndmask_b32_e64 v29, 0, v97, s[58:59]\n" \
  "v_cndmask_b32_e64 v24, 0, v96, s[60:61]\n" \
  "v_cndmask_b32_e64 v25, 0, v97, s[60:61]\n" \
  "v_mfma_f32_16x16x32_bf16 v[40:43], v[116:119], v[28:31], v[40:43]\n" \
  "v_permlane32_swap_b32 v64, v68\n" \
  "v_permlane32_swap_b32 v65, v69\n" \
  "v_permlane32_swap_b32 v66, v70\n" \
  "v_permlane32_swap_b32 v67, v71\n" \
  "v_pk_add_f32 v[64:65], v[64:65], v[68:69]\n" \
  "v_pk_add_f32 v[66:67], v[66:67], v[70:71]\n" \
  "v_cvt_pk_bf16_f32 v64, v64, v65\n" \
  "v_cvt_pk_bf16_f32 v65, v66, v67\n" \
  "v_mov_b64 v[66:67], 0\n" \
  "s_nop 0\n" \
  "s_nop 0\n" \
  "v_mfma_f32_16x16x32_bf16 v[20:23], v[64:67], v[28:31], v[20:23]\n" \
  "v_mfma_f32_16x16x32_bf16 v[16:19], v[64:67], v[24:27], v[16:19]\n" \
  "v_rcp_f32 v15, v40\n" \
  "v_mov_b64 v[72:73], 0\n" \
  "v_mov_b64 v[74:75], 0\n" \
  "v_mov_b64 v[76:77], 0\n" \
  "v_mov_b64 v[78:79], 0\n" \
  "v_mov_b64 v[80:81], 0\n" \
  "v_mov_b64 v[82:83], 0\n" \
  "v_mov_b64 v[84:85], 0\n" \
  "v_pk_mul_f32 v[16:17], v[16:17], v[14:15] op_sel:[0,1] op_sel_hi:[1,1]\n" \
  "v_pk_mul_f32 v[18:19], v[18:19], v[14:15] op_sel:[0,1] op_sel_hi:[1,1]\n" \
  "v_pk_mul_f32 v[20:21], v[20:21], v[14:15] op_sel:[0,1] op_sel_hi:[1,1]\n" \
  "v_pk_mul_f32 v[22:23], v[22:23], v[14:15] op_sel:[0,1] op_sel_hi:[1,1]\n" \
  "v_cvt_pk_bf16_f32 v16, v16, v17\n" \
  "v_cvt_pk_bf16_f32 v17, v18, v19\n" \
  "v_cvt_pk_bf16_f32 v20, v20, v21\n" \
  "v_cvt_pk_bf16_f32 v21, v22, v23\n" \
  "ds_write2_b64 v9, v[16:17], v[20:21] offset1:4\n" \
  "v_lshrrev_b32 v15, 23, v40\n" \
  "v_subrev_u32 v15, 67, v15\n" \
  "v_cmp_lt_u32 vcc, 120, v15\n" \
  "s_and_b64 vcc, vcc, s[56:57]\n" \
  "s_cbranch_vccnz RL_SLOW_%=\n" \
  "RL_BACK_%=:\n" \
  "v_mov_b64 v[86:87], 0\n" \
  "v_mov_b64 v[88:89], 0\n" \
  "v_mov_b64 v[90:91], 0\n" \
  "v_mov_b64 v[92:93], 0\n" \
  "v_mov_b64 v[94:95], 0\n" \
  "v_mov_b64 v[96:97], 0\n" \
  "v_mov_b64 v[98:99], 0\n" \
  "v_mov_b64 v[100:101], 0\n" \
  "v_mov_b64 v[102:103], 0\n" \
  "s_waitcnt lgkmcnt(0)\n" \
  "s_barrier\n" \
  "ds_read_b128 v[44:47], v10\n" \
  "ds_read_b128 v[32:35], v10 offset:128\n" \
  "s_add_u32 s74, s45, 65536\n" \
  "s_waitcnt lgkmcnt(1)\n" \
  "v_smfmac_f32_16x16x64_bf16 v[72:75], v[44:47], v[128:135], v1\n" \
  "v_smfmac_f32_16x16x64_bf16 v[76:79], v[44:47], v[144:151], v1\n" \
  "v_smfmac_f32_16x16x64_bf16 v[80:83], v[44:47], v[160:167], v1\n" \
  "v_smfmac_f32_16x16x64_bf16 v[84:87], v[44:47], v[176:183], v1\n" \
  "s_waitcnt lgkmcnt(0)\n" \
  "v_smfmac_f32_16x16x64_bf16 v[72:75], v[32:35], v[136:143], v1\n" \
  "v_smfmac_f32_16x16x64_bf16 v[76:79], v[32:35], v[152:159], v1\n" \
  "v_smfmac_f32_16x16x64_bf16 v[88:91], v[44:47], v[192:199], v1\n" \
  "v_smfmac_f32_16x16x64_bf16 v[92:95], v[44:47], v[208:215], v1\n" \
  "v_smfmac_f32_16x16x64_bf16 v[80:83], v[32:35], v[168:175], v1\n" \
  "v_permlane32_swap_b32 v72, v76\n" \
  "v_permlane32_swap_b32 v73, v77\n" \
  "v_smfmac_f32_16x16x64_bf16 v[84:87], v[32:35], v[184:191], v1\n" \
  "v_permlane32_swap_b32 v74, v78\n" \
  "v_permlane32_swap_b32 v75, v79\n" \
  "v_smfmac_f32_16x16x64_bf16 v[96:99], v[44:47], v[224:231], v1\n" \
  "v_pk_add_f32 v[72:73], v[72:73], v[76:77]\n" \
  "v_pk_add_f32 v[74:75], v[74:75], v[78:79]\n" \
  "v_smfmac_f32_16x16x64_bf16 v[100:103], v[44:47], v[240:247], v1\n" \
  "v_cvt_pk_bf16_f32 v36, v72, v73\n" \
  "v_cvt_pk_bf16_f32 v37, v74, v75\n" \
  "v_smfmac_f32_16x16x64_bf16 v[88:91], v[32:35], v[200:207], v1\n" \
  "v_permlane32_swap_b32 v80, v84\n" \
  "v_permlane32_swap_b32 v81, v85\n" \
  "v_smfmac_f32_16x16x64_bf16 v[92:95], v[32:35], v[216:223], v1\n" \
  "v_permlane32_swap_b32 v82, v86\n" \
  "v_permlane32_swap_b32 v83, v87\n" \
  "v_smfmac_f32_16x16x64_bf16 v[96:99], v[32:35], v[232:239], v1\n" \
  "v_pk_add_f32 v[80:81], v[80:81], v[84:85]\n" \
  "v_pk_add_f32 v[82:83], v[82:83], v[86:87]\n" \
  "v_smfmac_f32_16x16x64_bf16 v[100:103], v[32:35], v[248:255], v1\n" \
  "v_cvt_pk_bf16_f32 v38, v80, v81\n" \
  "v_cvt_pk_bf16_f32 v39, v82, v83\n" \
  "buffer_store_dwordx4 v[36:39], v11, s[40:43], s45 offen sc1\n" \
  "v_permlane32_swap_b32 v88, v92\n" \
  "v_permlane32_swap_b32 v89, v93\n" \
  "v_permlane32_swap_b32 v90, v94\n" \
  "v_permlane32_swap_b32 v91, v95\n" \
  "v_pk_add_f32 v[88:89], v[88:89], v[92:93]\n" \
  "v_pk_add_f32 v[90:91], v[90:91], v[94:95]\n" \
  "v_cvt_pk_bf16_f32 v40, v88, v89\n" \
  "v_cvt_pk_bf16_f32 v41, v90, v91\n" \
  "v_permlane32_swap_b32 v96, v100\n" \
  "v_permlane32_swap_b32 v97, v101\n" \
  "v_permlane32_swap_b32 v98, v102\n" \
  "v_permlane32_swap_b32 v99, v103\n" \
  "v_pk_add_f32 v[96:97], v[96:97], v[100:101]\n" \
  "v_pk_add_f32 v[98:99], v[98:99], v[102:103]\n" \
  "v_cvt_pk_bf16_f32 v42, v96, v97\n" \
  "v_cvt_pk_bf16_f32 v43, v98, v99\n" \
  "buffer_store_dwordx4 v[40:43], v11, s[40:43], s74 offen sc1\n" \
  "s_lshl_b32 s73, s77, 10\n" \
  "v_add_u32 v15, s73, v7\n" \
  "ds_read_b64_tr_b16 v[44:45], v15\n" \
  "ds_read_b64_tr_b16 v[46:47], v15 offset:512\n" \
  "v_lshrrev_b32 v28, 1, v0\n" \
  "v_and_b32 v29, 16, v0\n" \
  "v_mov_b32 v30, 0x3f80\n" \
  "v_lshlrev_b32 v30, v29, v30\n" \
  "v_cndmask_b32_e64 v80, 0, v30, s[62:63]\n" \
  "v_cndmask_b32_e64 v81, 0, v30, s[64:65]\n" \
  "v_cndmask_b32_e64 v82, 0, v30, s[66:67]\n" \
  "v_cndmask_b32_e64 v83, 0, v30, s[68:69]\n" \
  "s_waitcnt lgkmcnt(0)\n" \
  "s_lshl_b32 s39, s76, 8\n" \
  "s_lshl_b32 s74, s77, 6\n" \
  "s_add_u32 s39, s39, s74\n" \
  "s_add_u32 s39, s39, s47\n" \
  "s_mov_b32 s72, 0\n" \
  "buffer_store_dwordx2 v[64:65], v28, s[40:43], s51 offen sc1\n" \
  "buffer_store_dwordx4 v[44:47], v0, s[40:43], s50 offen sc1\n" \
  "s_add_u32 s50, s50, 131072\n" \
  "s_add_u32 s51, s51, 65536\n" \
  "v_mov_b64 v[24:25], -1\n" \
  "v_mov_b64 v[26:27], -1\n" \
  "s_xor_b32 s73, s46, 524288\n" \
  "buffer_store_dwordx4 v[24:27], v0, s[40:43], s73 offen sc1\n" \
  "buffer_store_dwordx4 v[24:27], v0, s[40:43], s73 offen offset:1024 sc1\n" \
  "s_mov_b32 s73, s85\n" \
  "RL_D1_%=:\n" \
  "s_sub_u32 s73, s73, 1\n" \
  "s_cbranch_scc0 RL_D1_%=\n" \
  "buffer_load_dwordx4 v[72:75], v0, s[40:43], s46 offen sc1\n" \
  "buffer_load_dwordx4 v[76:79], v0, s[40:43], s46 offen offset:1024 sc1\n" \
  "s_add_u32 s38, s44, 2\n" \
  "s_cmp_ge_u32 s36, s38\n" \
  "s_cbranch_scc1 RL_KVOK_%=\n" \
  "s_mov_b32 s72, 0\n" \
  "v_mov_b32 v31, 0\n" \
  "RL_KVP_%=:\n" \
  "buffer_load_dwordx2 v[100:101], v31, s[40:43], s32 offen sc1\n" \
  "s_add_u32 s72, s72, 1\n" \
  "s_cmp_gt_u32 s72, 65536\n" \
  "s_cbranch_scc1 RL_FAIL_%=\n" \
  "s_waitcnt vmcnt(0)\n" \
  "v_min_u32 v100, v100, v101\n" \
  "s_nop 1\n" \
  "v_readfirstlane_b32 s36, v100\n" \
  "s_nop 1\n" \
  "s_cmp_ge_u32 s36, s38\n" \
  "s_cbranch_scc1 RL_KVOK_%=\n" \
  "s_sleep 2\n" \
  "s_branch RL_KVP_%=\n" \
  "RL_KVOK_%=:\n" \
  "s_mov_b32 m0, s70\n" \
  "s_nop 0\n" \
  "buffer_load_dwordx4 v0, s[40:43], s48 offen offset:0 sc1 lds\n" \
  "buffer_load_dwordx4 v0, s[40:43], s48 offen offset:1024 sc1 lds\n" \
  "s_mov_b32 m0, s71\n" \
  "s_nop 0\n" \
  "buffer_load_dwordx4 v0, s[40:43], s49 offen offset:0 sc1 lds\n" \
  "buffer_load_dwordx4 v0, s[40:43], s49 offen offset:1024 sc1 lds\n" \
  "s_add_u32 s48, s48, 262144\n" \
  "s_add_u32 s49, s49, 262144\n" \
  "s_mov_b32 s72, 0\n" \
  "s_waitcnt vmcnt(4)\n" \
  "v_mfma_f32_16x16x32_bf16 v[84:87], v[80:83], v[72:75], 0\n" \
  "v_mfma_f32_16x16x32_bf16 v[84:87], v[80:83], v[76:79], v[84:87]\n" \
  "v_max3_u32 v15, v72, v73, v74\n" \
  "v_max3_u32 v15, v15, v75, v76\n" \
  "v_max3_u32 v15, v15, v77, v78\n" \
  "v_max_u32 v15, v15, v79\n" \
  "v_cmp_eq_u32 vcc, -1, v15\n" \
  "s_cbranch_vccz RL_QaOK_%=\n" \
  "RL_Qa_%=:\n" \
  "buffer_load_dwordx4 v[72:75], v0, s[40:43], s46 offen sc1\n" \
  "buffer_load_dwordx4 v[76:79], v0, s[40:43], s46 offen offset:1024 sc1\n" \
  "s_add_u32 s72, s72, 1\n" \
  "s_cmp_gt_u32 s72, 65536\n" \
  "s_cbranch_scc1 RL_FAIL_%=\n" \
  "s_waitcnt vmcnt(0)\n" \
  "v_mfma_f32_16x16x32_bf16 v[84:87], v[80:83], v[72:75], 0\n" \
  "v_mfma_f32_16x16x32_bf16 v[84:87], v[80:83], v[76:79], v[84:87]\n" \
  "v_max3_u32 v15, v72, v73, v74\n" \
  "v_max3_u32 v15, v15, v75, v76\n" \
  "v_max3_u32 v15, v15, v77, v78\n" \
  "v_max_u32 v15, v15, v79\n" \
  "v_cmp_eq_u32 vcc, -1, v15\n" \
  "s_cbranch_vccnz RL_Qa_%=\n" \
  "RL_QaOK_%=:\n" \
  "s_nop 1\n" \
  "v_add_f32_dpp v84, v84, v84 row_ror:8 row_mask:0xf bank_mask:0xf\n" \
  "v_add_f32_dpp v85, v85, v85 row_ror:8 row_mask:0xf bank_mask:0xf\n" \
  "v_add_f32_dpp v86, v86, v86 row_ror:8 row_mask:0xf bank_mask:0xf\n" \
  "v_add_f32_dpp v87, v87, v87 row_ror:8 row_mask:0xf bank_mask:0xf\n" \
  "v_add_f32_dpp v84, v84, v84 row_ror:4 row_mask:0xf bank_mask:0xf\n" \
  "v_add_f32_dpp v85, v85, v85 row_ror:4 row_mask:0xf bank_mask:0xf\n" \
  "v_add_f32_dpp v86, v86, v86 row_ror:4 row_mask:0xf bank_mask:0xf\n" \
  "v_add_f32_dpp v87, v87, v87 row_ror:4 row_mask:0xf bank_mask:0xf\n" \
  "v_cvt_pk_bf16_f32 v84, v84, v85\n" \
  "v_cvt_pk_bf16_f32 v85, v86, v87\n" \
  "s_mov_b64 exec, s[54:55]\n" \
  "buffer_store_dwordx2 v[84:85], v12, s[40:43], s39 offen sc1\n" \
  "s_mov_b64 exec, -1\n" \
  "s_add_u32 s73, s44, 1\n" \
  "v_mov_b32 v29, s73\n" \
  "v_mov_b32 v30, 0\n" \
  "s_mov_b64 exec, 1\n" \
  "buffer_store_dword v29, v30, s[40:43], s33 offen sc1\n" \
  "s_mov_b64 exec, -1\n" \
  "s_mov_b32 s72, 0\n" \
  "s_lshl_b32 s74, s77, 11\n" \
  "s_add_u32 s47, s47, s74\n" \
  "v_mov_b64 v[48:49], 0\n" \
  "v_mov_b64 v[50:51], 0\n" \
  "v_mov_b64 v[52:53], 0\n" \
  "v_mov_b64 v[54:55], 0\n" \
  "v_mov_b64 v[56:57], 0\n" \
  "v_mov_b64 v[58:59], 0\n" \
  "v_mov_b64 v[60:61], 0\n" \
  "v_mov_b64 v[62:63], 0\n" \
  "s_mov_b32 s73, s86\n" \
  "RL_D2_%=:\n" \
  "s_sub_u32 s73, s73, 1\n" \
  "s_cbranch_scc0 RL_D2_%=\n" \
  "buffer_load_dwordx4 v[32:35], v0, s[40:43], s47 offen sc1\n" \
  "buffer_load_dwordx4 v[36:39], v0, s[40:43], s47 offen offset:1024 sc1\n" \
  "ds_read_b128 v[64:67], v3\n" \
  "ds_read_b128 v[68:71], v3 offset:1024\n" \
  "ds_read_b128 v[72:75], v3 offset:2048\n" \
  "ds_read_b128 v[76:79], v3 offset:3072\n" \
  "ds_read_b128 v[80:83], v3 offset:4096\n" \
  "ds_read_b128 v[84:87], v3 offset:5120\n" \
  "ds_read_b128 v[88:91], v3 offset:6144\n" \
  "ds_read_b128 v[92:95], v3 offset:7168\n" \
  "ds_read_b128 v[96:99], v3 offset:8192\n" \
  "ds_read_b128 v[100:103], v3 offset:9216\n" \
  "ds_read_b128 v[104:107], v3 offset:10240\n" \
  "ds_read_b128 v[108:111], v3 offset:11264\n" \
  "ds_read_b128 v[112:115], v3 offset:12288\n" \
  "ds_read_b128 v[116:119], v3 offset:13312\n" \
  "ds_read_b128 v[120:123], v3 offset:14336\n" \
  "ds_read_b128 v[124:127], v3 offset:15360\n" \
  "s_waitcnt vmcnt(0)\n" \
  "ds_write_b128 v8, v[32:35]\n" \
  "ds_write_b128 v8, v[36:39] offset:1024\n" \
  "v_max3_u32 v15, v32, v33, v34\n" \
  "v_max3_u32 v15, v15, v35, v36\n" \
  "v_max3_u32 v15, v15, v37, v38\n" \
  "v_max_u32 v15, v15, v39\n" \
  "v_cmp_eq_u32 vcc, -1, v15\n" \
  "s_cbranch_vccz RL_QbOK_%=\n" \
  "RL_Qb_%=:\n" \
  "buffer_load_dwordx4 v[32:35], v0, s[40:43], s47 offen sc1\n" \
  "buffer_load_dwordx4 v[36:39], v0, s[40:43], s47 offen offset:1024 sc1\n" \
  "s_add_u32 s72, s72, 1\n" \
  "s_cmp_gt_u32 s72, 65536\n" \
  "s_cbranch_scc1 RL_FAIL_%=\n" \
  "s_waitcnt vmcnt(0)\n" \
  "ds_write_b128 v8, v[32:35]\n" \
  "ds_write_b128 v8, v[36:39] offset:1024\n" \
  "v_max3_u32 v15, v32, v33, v34\n" \
  "v_max3_u32 v15, v15, v35, v36\n" \
  "v_max3_u32 v15, v15, v37, v38\n" \
  "v_max_u32 v15, v15, v39\n" \
  "v_cmp_eq_u32 vcc, -1, v15\n" \
  "s_cbranch_vccnz RL_Qb_%=\n" \
  "RL_QbOK_%=:\n" \
  "s_add_u32 s44, s44, 1\n" \
  "s_xor_b32 s45, s45, 524288\n" \
  "s_xor_b32 s46, s46, 524288\n" \
  "s_lshl_b32 s74, s77, 11\n" \
  "s_sub_u32 s47, s47, s74\n" \
  "s_add_u32 s47, s47, 16384\n" \
  "s_waitcnt lgkmcnt(0)\n" \
  "s_barrier\n" \
  "s_branch RL_LOOP_%=\n" \
  "RL_SLOW_%=:\n" \
  "s_nop 7\n" \
  "v_max3_f32 v15, v108, v109, v110\n" \
  "v_max3_f32 v15, v15, v111, v112\n" \
  "v_max3_f32 v15, v15, v113, v114\n" \
  "v_max3_f32 v15, v15, v115, v104\n" \
  "v_max3_f32 v15, v15, v105, v106\n" \
  "v_max_f32 v15, v15, v107\n" \
  "v_mov_b32 v28, v15\n" \
  "s_nop 0\n" \
  "s_nop 0\n" \
  "v_permlane16_swap_b32 v15, v28\n" \
  "v_max_f32 v15, v15, v28\n" \
  "v_mov_b32 v28, v15\n" \
  "s_nop 0\n" \
  "s_nop 0\n" \
  "v_permlane32_swap_b32 v15, v28\n" \
  "v_max_f32 v15, v15, v28\n" \
  "v_sub_f32 v108, v108, v15\n" \
  "v_sub_f32 v109, v109, v15\n" \
  "v_sub_f32 v110, v110, v15\n" \
  "v_sub_f32 v111, v111, v15\n" \
  "v_sub_f32 v112, v112, v15\n" \
  "v_sub_f32 v113, v113, v15\n" \
  "v_sub_f32 v114, v114, v15\n" \
  "v_sub_f32 v115, v115, v15\n" \
  "v_sub_f32 v104, v104, v15\n" \
  "v_sub_f32 v105, v105, v15\n" \
  "v_sub_f32 v106, v106, v15\n" \
  "v_sub_f32 v107, v107, v15\n" \
  "s_waitcnt lgkmcnt(0)\n" \
  "s_nop 1\n" \
  "v_exp_f32 v80, v108\n" \
  "v_exp_f32 v81, v109\n" \
  "v_exp_f32 v82, v110\n" \
  "v_exp_f32 v83, v111\n" \
  "v_exp_f32 v84, v112\n" \
  "v_exp_f32 v85, v113\n" \
  "v_exp_f32 v86, v114\n" \
  "v_exp_f32 v87, v115\n" \
  "v_cvt_pk_bf16_f32 v92, v80, v81\n" \
  "v_cvt_pk_bf16_f32 v93, v82, v83\n" \
  "v_cvt_pk_bf16_f32 v94, v84, v85\n" \
  "v_cvt_pk_bf16_f32 v95, v86, v87\n" \
  "v_exp_f32 v88, v104\n" \
  "v_exp_f32 v89, v105\n" \
  "v_mfma_f32_16x16x32_bf16 v[16:19], v[120:123], v[92:95], 0\n" \
  "v_exp_f32 v90, v106\n" \
  "v_mfma_f32_16x16x32_bf16 v[20:23], v[124:127], v[92:95], 0\n" \
  "v_exp_f32 v91, v107\n" \
  "v_mfma_f32_16x16x32_bf16 v[40:43], v[116:119], v[92:95], 0\n" \
  "v_mov_b64 v[26:27], 0\n" \
  "v_mov_b64 v[30:31], 0\n" \
  "v_cvt_pk_bf16_f32 v96, v88, v89\n" \
  "v_cvt_pk_bf16_f32 v97, v90, v91\n" \
  "v_cndmask_b32_e64 v28, 0, v96, s[58:59]\n" \
  "v_cndmask_b32_e64 v29, 0, v97, s[58:59]\n" \
  "v_cndmask_b32_e64 v24, 0, v96, s[60:61]\n" \
  "v_cndmask_b32_e64 v25, 0, v97, s[60:61]\n" \
  "v_mfma_f32_16x16x32_bf16 v[40:43], v[116:119], v[28:31], v[40:43]\n" \
  "s_nop 1\n" \
  "v_mfma_f32_16x16x32_bf16 v[20:23], v[64:67], v[28:31], v[20:23]\n" \
  "v_mfma_f32_16x16x32_bf16 v[16:19], v[64:67], v[24:27], v[16:19]\n" \
  "s_nop 1\n" \
  "v_rcp_f32 v15, v40\n" \
  "v_mov_b64 v[72:73], 0\n" \
  "v_mov_b64 v[74:75], 0\n" \
  "v_mov_b64 v[76:77], 0\n" \
  "v_mov_b64 v[78:79], 0\n" \
  "v_mov_b64 v[80:81], 0\n" \
  "v_mov_b64 v[82:83], 0\n" \
  "v_mov_b64 v[84:85], 0\n" \
  "v_pk_mul_f32 v[16:17], v[16:17], v[14:15] op_sel:[0,1] op_sel_hi:[1,1]\n" \
  "v_pk_mul_f32 v[18:19], v[18:19], v[14:15] op_sel:[0,1] op_sel_hi:[1,1]\n" \
  "v_pk_mul_f32 v[20:21], v[20:21], v[14:15] op_sel:[0,1] op_sel_hi:[1,1]\n" \
  "v_pk_mul_f32 v[22:23], v[22:23], v[14:15] op_sel:[0,1] op_sel_hi:[1,1]\n" \
  "v_cvt_pk_bf16_f32 v16, v16, v17\n" \
  "v_cvt_pk_bf16_f32 v17, v18, v19\n" \
  "v_cvt_pk_bf16_f32 v20, v20, v21\n" \
  "v_cvt_pk_bf16_f32 v21, v22, v23\n" \
  "ds_write2_b64 v9, v[16:17], v[20:21] offset1:4\n" \
  "s_branch RL_BACK_%=\n" \
  "RL_LAST_%=:\n" \
  "s_nop 7\n" \
  "v_permlane32_swap_b32 v64, v68\n" \
  "v_permlane32_swap_b32 v65, v69\n" \
  "v_permlane32_swap_b32 v66, v70\n" \
  "v_permlane32_swap_b32 v67, v71\n" \
  "v_pk_add_f32 v[64:65], v[64:65], v[68:69]\n" \
  "v_pk_add_f32 v[66:67], v[66:67], v[70:71]\n" \
  "v_cvt_pk_bf16_f32 v64, v64, v65\n" \
  "v_cvt_pk_bf16_f32 v65, v66, v67\n" \
  "v_mov_b64 v[66:67], 0\n" \
  "s_lshl_b32 s73, s77, 10\n" \
  "v_add_u32 v15, s73, v7\n" \
  "ds_read_b64_tr_b16 v[44:45], v15\n" \
  "ds_read_b64_tr_b16 v[46:47], v15 offset:512\n" \
  "v_lshrrev_b32 v28, 1, v0\n" \
  "buffer_store_dwordx2 v[64:65], v28, s[40:43], s51 offen sc1\n" \
  "s_waitcnt lgkmcnt(0)\n" \
  "buffer_store_dwordx4 v[44:47], v0, s[40:43], s50 offen sc1\n" \
  "v_mov_b64 v[24:25], -1\n" \
  "v_mov_b64 v[26:27], -1\n" \
  "s_xor_b32 s73, s46, 524288\n" \
  "s_nop 0\n" \
  "buffer_store_dwordx4 v[24:27], v0, s[40:43], s73 offen sc1\n" \
  "buffer_store_dwordx4 v[24:27], v0, s[40:43], s73 offen offset:1024 sc1\n" \
  "s_nop 1\n" \
  "v_mov_b32 v29, 64\n" \
  "v_mov_b32 v30, 0\n" \
  "s_waitcnt vmcnt(0)\n" \
  "s_mov_b64 exec, 1\n" \
  "buffer_store_dword v29, v30, s[40:43], s33 offen sc1\n" \
  "s_mov_b64 exec, -1\n" \
  "s_branch RL_END_%=\n" \
  "RL_FAIL_%=:\n" \
  "v_mov_b32 v15, 1\n" \
  "v_mov_b32 v28, 0\n" \
  "s_nop 1\n" \
  "buffer_store_dword v15, v28, s[40:43], 0 offen sc1\n" \
  "RL_END_%=:\n" \
  "s_waitcnt vmcnt(0) lgkmcnt(0)\n" \
  ""

#define ATT_ASM \
  "s_nop 1\n" \
  "v_mov_b32 v88, v68\n" \
  "v_mov_b32 v89, v68\n" \
  "v_mov_b32 v90, v68\n" \
  "v_mov_b32 v91, v68\n" \
  "v_mov_b32 v92, v68\n" \
  "v_mov_b32 v93, v68\n" \
  "v_mov_b32 v94, v68\n" \
  "v_mov_b32 v95, v68\n" \
  "v_mov_b32 v96, v68\n" \
  "v_mov_b32 v97, v68\n" \
  "v_mov_b32 v98, v68\n" \
  "v_mov_b32 v99, v68\n" \
  "v_mov_b32 v100, v68\n" \
  "v_mov_b32 v101, v68\n" \
  "v_mov_b32 v102, v68\n" \
  "v_mov_b32 v103, v68\n" \
  "ds_read_b128 v[144:147], v69\n" \
  "ds_read_b128 v[148:151], v69 offset:1024\n" \
  "ds_read_b128 v[152:155], v70\n" \
  "ds_read_b128 v[156:159], v70 offset:512\n" \
  "v_mov_b64 v[72:73], 0\n" \
  "v_mov_b64 v[74:75], 0\n" \
  "v_mov_b64 v[76:77], 0\n" \
  "v_mov_b64 v[78:79], 0\n" \
  "v_mov_b64 v[80:81], 0\n" \
  "v_mov_b64 v[82:83], 0\n" \
  "v_mov_b64 v[84:85], 0\n" \
  "v_mov_b64 v[86:87], 0\n" \
  "s_waitcnt lgkmcnt(3)\n" \
  "v_mfma_f32_32x32x16_bf16 v[104:119], v[144:147], v[64:67], v[88:103]\n" \
  "s_waitcnt lgkmcnt(2)\n" \
  "v_mfma_f32_32x32x16_bf16 v[120:135], v[148:151], v[64:67], v[88:103]\n" \
  "v_add_u32 v70, v70, v71\n" \
  "ds_read_b128 v[160:163], v70\n" \
  "ds_read_b128 v[164:167], v70 offset:512\n" \
  "ds_read_b128 v[144:147], v69 offset:2048\n" \
  "s_nop 5\n" \
  "v_exp_f32 v104, v104\n" \
  "v_exp_f32 v105, v105\n" \
  "v_exp_f32 v106, v106\n" \
  "v_exp_f32 v107, v107\n" \
  "v_exp_f32 v108, v108\n" \
  "v_exp_f32 v109, v109\n" \
  "v_exp_f32 v110, v110\n" \
  "v_exp_f32 v111, v111\n" \
  "v_cvt_pk_bf16_f32 v136, v104, v105\n" \
  "v_cvt_pk_bf16_f32 v137, v106, v107\n" \
  "v_cvt_pk_bf16_f32 v138, v108, v109\n" \
  "v_cvt_pk_bf16_f32 v139, v110, v111\n" \
  "v_exp_f32 v112, v112\n" \
  "v_exp_f32 v113, v113\n" \
  "s_waitcnt lgkmcnt(4)\n" \
  "v_mfma_f32_32x32x16_bf16 v[72:87], v[152:155], v[136:139], v[72:87]\n" \
  "v_exp_f32 v114, v114\n" \
  "v_exp_f32 v115, v115\n" \
  "v_exp_f32 v116, v116\n" \
  "v_exp_f32 v117, v117\n" \
  "v_exp_f32 v118, v118\n" \
  "v_exp_f32 v119, v119\n" \
  "v_cvt_pk_bf16_f32 v140, v112, v113\n" \
  "v_cvt_pk_bf16_f32 v141, v114, v115\n" \
  "v_cvt_pk_bf16_f32 v142, v116, v117\n" \
  "v_cvt_pk_bf16_f32 v143, v118, v119\n" \
  "s_waitcnt lgkmcnt(3)\n" \
  "s_nop 0\n" \
  "v_mfma_f32_32x32x16_bf16 v[72:87], v[156:159], v[140:143], v[72:87]\n" \
  "s_waitcnt lgkmcnt(0)\n" \
  "v_mfma_f32_32x32x16_bf16 v[104:119], v[144:147], v[64:67], v[88:103]\n" \
  "v_add_u32 v70, v70, v71\n" \
  "ds_read_b128 v[152:155], v70\n" \
  "ds_read_b128 v[156:159], v70 offset:512\n" \
  "ds_read_b128 v[148:151], v69 offset:3072\n" \
  "v_exp_f32 v120, v120\n" \
  "v_exp_f32 v121, v121\n" \
  "v_exp_f32 v122, v122\n" \
  "v_exp_f32 v123, v123\n" \
  "v_exp_f32 v124, v124\n" \
  "v_exp_f32 v125, v125\n" \
  "v_exp_f32 v126, v126\n" \
  "v_exp_f32 v127, v127\n" \
  "v_cvt_pk_bf16_f32 v136, v120, v121\n" \
  "v_cvt_pk_bf16_f32 v137, v122, v123\n" \
  "v_cvt_pk_bf16_f32 v138, v124, v125\n" \
  "v_cvt_pk_bf16_f32 v139, v126, v127\n" \
  "v_exp_f32 v128, v128\n" \
  "v_exp_f32 v129, v129\n" \
  "v_mfma_f32_32x32x16_bf16 v[72:87], v[160:163], v[136:139], v[72:87]\n" \
  "v_exp_f32 v130, v130\n" \
  "v_exp_f32 v131, v131\n" \
  "v_exp_f32 v132, v132\n" \
  "v_exp_f32 v133, v133\n" \
  "v_exp_f32 v134, v134\n" \
  "v_exp_f32 v135, v135\n" \
  "v_cvt_pk_bf16_f32 v140, v128, v129\n" \
  "v_cvt_pk_bf16_f32 v141, v130, v131\n" \
  "v_cvt_pk_bf16_f32 v142, v132, v133\n" \
  "v_cvt_pk_bf16_f32 v143, v134, v135\n" \
  "s_nop 0\n" \
  "s_nop 0\n" \
  "v_mfma_f32_32x32x16_bf16 v[72:87], v[164:167], v[140:143], v[72:87]\n" \
  "s_waitcnt lgkmcnt(0)\n" \
  "v_mfma_f32_32x32x16_bf16 v[120:135], v[148:151], v[64:67], v[88:103]\n" \
  "v_add_u32 v70, v70, v71\n" \
  "ds_read_b128 v[160:163], v70\n" \
  "ds_read_b128 v[164:167], v70 offset:512\n" \
  "ds_read_b128 v[144:147], v69 offset:4096\n" \
  "v_exp_f32 v104, v104\n" \
  "v_exp_f32 v105, v105\n" \
  "v_exp_f32 v106, v106\n" \
  "v_exp_f32 v107, v107\n" \
  "v_exp_f32 v108, v108\n" \
  "v_exp_f32 v109, v109\n" \
  "v_exp_f32 v110, v110\n" \
  "v_exp_f32 v111, v111\n" \
  "v_cvt_pk_bf16_f32 v136, v104, v105\n" \
  "v_cvt_pk_bf16_f32 v137, v106, v107\n" \
  "v_cvt_pk_bf16_f32 v138, v108, v109\n" \
  "v_cvt_pk_bf16_f32 v139, v110, v111\n" \
  "v_exp_f32 v112, v112\n" \
  "v_exp_f32 v113, v113\n" \
  "v_mfma_f32_32x32x16_bf16 v[72:87], v[152:155], v[136:139], v[72:87]\n" \
  "v_exp_f32 v114, v114\n" \
  "v_exp_f32 v115, v115\n" \
  "v_exp_f32 v116, v116\n" \
  "v_exp_f32 v117, v117\n" \
  "v_exp_f32 v118, v118\n" \
  "v_exp_f32 v119, v119\n" \
  "v_cvt_pk_bf16_f32 v140, v112, v113\n" \
  "v_cvt_pk_bf16_f32 v141, v114, v115\n" \
  "v_cvt_pk_bf16_f32 v142, v116, v117\n" \
  "v_cvt_pk_bf16_f32 v143, v118, v119\n" \
  "s_nop 0\n" \
  "s_nop 0\n" \
  "v_mfma_f32_32x32x16_bf16 v[72:87], v[156:159], v[140:143], v[72:87]\n" \
  "s_waitcnt lgkmcnt(0)\n" \
  "v_mfma_f32_32x32x16_bf16 v[104:119], v[144:147], v[64:67], v[88:103]\n" \
  "v_add_u32 v70, v70, v71\n" \
  "ds_read_b128 v[152:155], v70\n" \
  "ds_read_b128 v[156:159], v70 offset:512\n" \
  "ds_read_b128 v[148:151], v69 offset:5120\n" \
  "v_exp_f32 v120, v120\n" \
  "v_exp_f32 v121, v121\n" \
  "v_exp_f32 v122, v122\n" \
  "v_exp_f32 v123, v123\n" \
  "v_exp_f32 v124, v124\n" \
  "v_exp_f32 v125, v125\n" \
  "v_exp_f32 v126, v126\n" \
  "v_exp_f32 v127, v127\n" \
  "v_cvt_pk_bf16_f32 v136, v120, v121\n" \
  "v_cvt_pk_bf16_f32 v137, v122, v123\n" \
  "v_cvt_pk_bf16_f32 v138, v124, v125\n" \
  "v_cvt_pk_bf16_f32 v139, v126, v127\n" \
  "v_exp_f32 v128, v128\n" \
  "v_exp_f32 v129, v129\n" \
  "v_mfma_f32_32x32x16_bf16 v[72:87], v[160:163], v[136:139], v[72:87]\n" \
  "v_exp_f32 v130, v130\n" \
  "v_exp_f32 v131, v131\n" \
  "v_exp_f32 v132, v132\n" \
  "v_exp_f32 v133, v133\n" \
  "v_exp_f32 v134, v134\n" \
  "v_exp_f32 v135, v135\n" \
  "v_cvt_pk_bf16_f32 v140, v128, v129\n" \
  "v_cvt_pk_bf16_f32 v141, v130, v131\n" \
  "v_cvt_pk_bf16_f32 v142, v132, v133\n" \
  "v_cvt_pk_bf16_f32 v143, v134, v135\n" \
  "s_nop 0\n" \
  "s_nop 0\n" \
  "v_mfma_f32_32x32x16_bf16 v[72:87], v[164:167], v[140:143], v[72:87]\n" \
  "s_waitcnt lgkmcnt(0)\n" \
  "v_mfma_f32_32x32x16_bf16 v[120:135], v[148:151], v[64:67], v[88:103]\n" \
  "v_add_u32 v70, v70, v71\n" \
  "ds_read_b128 v[160:163], v70\n" \
  "ds_read_b128 v[164:167], v70 offset:512\n" \
  "ds_read_b128 v[144:147], v69 offset:6144\n" \
  "v_exp_f32 v104, v104\n" \
  "v_exp_f32 v105, v105\n" \
  "v_exp_f32 v106, v106\n" \
  "v_exp_f32 v107, v107\n" \
  "v_exp_f32 v108, v108\n" \
  "v_exp_f32 v109, v109\n" \
  "v_exp_f32 v110, v110\n" \
  "v_exp_f32 v111, v111\n" \
  "v_cvt_pk_bf16_f32 v136, v104, v105\n" \
  "v_cvt_pk_bf16_f32 v137, v106, v107\n" \
  "v_cvt_pk_bf16_f32 v138, v108, v109\n" \
  "v_cvt_pk_bf16_f32 v139, v110, v111\n" \
  "v_exp_f32 v112, v112\n" \
  "v_exp_f32 v113, v113\n" \
  "v_mfma_f32_32x32x16_bf16 v[72:87], v[152:155], v[136:139], v[72:87]\n" \
  "v_exp_f32 v114, v114\n" \
  "v_exp_f32 v115, v115\n" \
  "v_exp_f32 v116, v116\n" \
  "v_exp_f32 v117, v117\n" \
  "v_exp_f32 v118, v118\n" \
  "v_exp_f32 v119, v119\n" \
  "v_cvt_pk_bf16_f32 v140, v112, v113\n" \
  "v_cvt_pk_bf16_f32 v141, v114, v115\n" \
  "v_cvt_pk_bf16_f32 v142, v116, v117\n" \
  "v_cvt_pk_bf16_f32 v143, v118, v119\n" \
  "s_nop 0\n" \
  "s_nop 0\n" \
  "v_mfma_f32_32x32x16_bf16 v[72:87], v[156:159], v[140:143], v[72:87]\n" \
  "s_waitcnt lgkmcnt(0)\n" \
  "v_mfma_f32_32x32x16_bf16 v[104:119], v[144:147], v[64:67], v[88:103]\n" \
  "v_add_u32 v70, v70, v71\n" \
  "ds_read_b128 v[152:155], v70\n" \
  "ds_read_b128 v[156:159], v70 offset:512\n" \
  "ds_read_b128 v[148:151], v69 offset:7168\n" \
  "v_exp_f32 v120, v120\n" \
  "v_exp_f32 v121, v121\n" \
  "v_exp_f32 v122, v122\n" \
  "v_exp_f32 v123, v123\n" \
  "v_exp_f32 v124, v124\n" \
  "v_exp_f32 v125, v125\n" \
  "v_exp_f32 v126, v126\n" \
  "v_exp_f32 v127, v127\n" \
  "v_cvt_pk_bf16_f32 v136, v120, v121\n" \
  "v_cvt_pk_bf16_f32 v137, v122, v123\n" \
  "v_cvt_pk_bf16_f32 v138, v124, v125\n" \
  "v_cvt_pk_bf16_f32 v139, v126, v127\n" \
  "v_exp_f32 v128, v128\n" \
  "v_exp_f32 v129, v129\n" \
  "v_mfma_f32_32x32x16_bf16 v[72:87], v[160:163], v[136:139], v[72:87]\n" \
  "v_exp_f32 v130, v130\n" \
  "v_exp_f32 v131, v131\n" \
  "v_exp_f32 v132, v132\n" \
  "v_exp_f32 v133, v133\n" \
  "v_exp_f32 v134, v134\n" \
  "v_exp_f32 v135, v135\n" \
  "v_cvt_pk_bf16_f32 v140, v128, v129\n" \
  "v_cvt_pk_bf16_f32 v141, v130, v131\n" \
  "v_cvt_pk_bf16_f32 v142, v132, v133\n" \
  "v_cvt_pk_bf16_f32 v143, v134, v135\n" \
  "s_nop 0\n" \
  "s_nop 0\n" \
  "v_mfma_f32_32x32x16_bf16 v[72:87], v[164:167], v[140:143], v[72:87]\n" \
  "s_waitcnt lgkmcnt(0)\n" \
  "v_mfma_f32_32x32x16_bf16 v[120:135], v[148:151], v[64:67], v[88:103]\n" \
  "v_add_u32 v70, v70, v71\n" \
  "ds_read_b128 v[160:163], v70\n" \
  "ds_read_b128 v[164:167], v70 offset:512\n" \
  "ds_read_b128 v[144:147], v69 offset:8192\n" \
  "v_exp_f32 v104, v104\n" \
  "v_exp_f32 v105, v105\n" \
  "v_exp_f32 v106, v106\n" \
  "v_exp_f32 v107, v107\n" \
  "v_exp_f32 v108, v108\n" \
  "v_exp_f32 v109, v109\n" \
  "v_exp_f32 v110, v110\n" \
  "v_exp_f32 v111, v111\n" \
  "v_cvt_pk_bf16_f32 v136, v104, v105\n" \
  "v_cvt_pk_bf16_f32 v137, v106, v107\n" \
  "v_cvt_pk_bf16_f32 v138, v108, v109\n" \
  "v_cvt_pk_bf16_f32 v139, v110, v111\n" \
  "v_exp_f32 v112, v112\n" \
  "v_exp_f32 v113, v113\n" \
  "v_mfma_f32_32x32x16_bf16 v[72:87], v[152:155], v[136:139], v[72:87]\n" \
  "v_exp_f32 v114, v114\n" \
  "v_exp_f32 v115, v115\n" \
  "v_exp_f32 v116, v116\n" \
  "v_exp_f32 v117, v117\n" \
  "v_exp_f32 v118, v118\n" \
  "v_exp_f32 v119, v119\n" \
  "v_cvt_pk_bf16_f32 v140, v112, v113\n" \
  "v_cvt_pk_bf16_f32 v141, v114, v115\n" \
  "v_cvt_pk_bf16_f32 v142, v116, v117\n" \
  "v_cvt_pk_bf16_f32 v143, v118, v119\n" \
  "s_nop 0\n" \
  "s_nop 0\n" \
  "v_mfma_f32_32x32x16_bf16 v[72:87], v[156:159], v[140:143], v[72:87]\n" \
  "s_waitcnt lgkmcnt(0)\n" \
  "v_mfma_f32_32x32x16_bf16 v[104:119], v[144:147], v[64:67], v[88:103]\n" \
  "v_add_u32 v70, v70, v71\n" \
  "ds_read_b128 v[152:155], v70\n" \
  "ds_read_b128 v[156:159], v70 offset:512\n" \
  "ds_read_b128 v[148:151], v69 offset:9216\n" \
  "v_exp_f32 v120, v120\n" \
  "v_exp_f32 v121, v121\n" \
  "v_exp_f32 v122, v122\n" \
  "v_exp_f32 v123, v123\n" \
  "v_exp_f32 v124, v124\n" \
  "v_exp_f32 v125, v125\n" \
  "v_exp_f32 v126, v126\n" \
  "v_exp_f32 v127, v127\n" \
  "v_cvt_pk_bf16_f32 v136, v120, v121\n" \
  "v_cvt_pk_bf16_f32 v137, v122, v123\n" \
  "v_cvt_pk_bf16_f32 v138, v124, v125\n" \
  "v_cvt_pk_bf16_f32 v139, v126, v127\n" \
  "v_exp_f32 v128, v128\n" \
  "v_exp_f32 v129, v129\n" \
  "v_mfma_f32_32x32x16_bf16 v[72:87], v[160:163], v[136:139], v[72:87]\n" \
  "v_exp_f32 v130, v130\n" \
  "v_exp_f32 v131, v131\n" \
  "v_exp_f32 v132, v132\n" \
  "v_exp_f32 v133, v133\n" \
  "v_exp_f32 v134, v134\n" \
  "v_exp_f32 v135, v135\n" \
  "v_cvt_pk_bf16_f32 v140, v128, v129\n" \
  "v_cvt_pk_bf16_f32 v141, v130, v131\n" \
  "v_cvt_pk_bf16_f32 v142, v132, v133\n" \
  "v_cvt_pk_bf16_f32 v143, v134, v135\n" \
  "s_nop 0\n" \
  "s_nop 0\n" \
  "v_mfma_f32_32x32x16_bf16 v[72:87], v[164:167], v[140:143], v[72:87]\n" \
  "s_waitcnt lgkmcnt(0)\n" \
  "v_mfma_f32_32x32x16_bf16 v[120:135], v[148:151], v[64:67], v[88:103]\n" \
  "v_add_u32 v70, v70, v71\n" \
  "ds_read_b128 v[160:163], v70\n" \
  "ds_read_b128 v[164:167], v70 offset:512\n" \
  "ds_read_b128 v[144:147], v69 offset:10240\n" \
  "v_exp_f32 v104, v104\n" \
  "v_exp_f32 v105, v105\n" \
  "v_exp_f32 v106, v106\n" \
  "v_exp_f32 v107, v107\n" \
  "v_exp_f32 v108, v108\n" \
  "v_exp_f32 v109, v109\n" \
  "v_exp_f32 v110, v110\n" \
  "v_exp_f32 v111, v111\n" \
  "v_cvt_pk_bf16_f32 v136, v104, v105\n" \
  "v_cvt_pk_bf16_f32 v137, v106, v107\n" \
  "v_cvt_pk_bf16_f32 v138, v108, v109\n" \
  "v_cvt_pk_bf16_f32 v139, v110, v111\n" \
  "v_exp_f32 v112, v112\n" \
  "v_exp_f32 v113, v113\n" \
  "v_mfma_f32_32x32x16_bf16 v[72:87], v[152:155], v[136:139], v[72:87]\n" \
  "v_exp_f32 v114, v114\n" \
  "v_exp_f32 v115, v115\n" \
  "v_exp_f32 v116, v116\n" \
  "v_exp_f32 v117, v117\n" \
  "v_exp_f32 v118, v118\n" \
  "v_exp_f32 v119, v119\n" \
  "v_cvt_pk_bf16_f32 v140, v112, v113\n" \
  "v_cvt_pk_bf16_f32 v141, v114, v115\n" \
  "v_cvt_pk_bf16_f32 v142, v116, v117\n" \
  "v_cvt_pk_bf16_f32 v143, v118, v119\n" \
  "s_nop 0\n" \
  "s_nop 0\n" \
  "v_mfma_f32_32x32x16_bf16 v[72:87], v[156:159], v[140:143], v[72:87]\n" \
  "s_waitcnt lgkmcnt(0)\n" \
  "v_mfma_f32_32x32x16_bf16 v[104:119], v[144:147], v[64:67], v[88:103]\n" \
  "v_add_u32 v70, v70, v71\n" \
  "ds_read_b128 v[152:155], v70\n" \
  "ds_read_b128 v[156:159], v70 offset:512\n" \
  "ds_read_b128 v[148:151], v69 offset:11264\n" \
  "v_exp_f32 v120, v120\n" \
  "v_exp_f32 v121, v121\n" \
  "v_exp_f32 v122, v122\n" \
  "v_exp_f32 v123, v123\n" \
  "v_exp_f32 v124, v124\n" \
  "v_exp_f32 v125, v125\n" \
  "v_exp_f32 v126, v126\n" \
  "v_exp_f32 v127, v127\n" \
  "v_cvt_pk_bf16_f32 v136, v120, v121\n" \
  "v_cvt_pk_bf16_f32 v137, v122, v123\n" \
  "v_cvt_pk_bf16_f32 v138, v124, v125\n" \
  "v_cvt_pk_bf16_f32 v139, v126, v127\n" \
  "v_exp_f32 v128, v128\n" \
  "v_exp_f32 v129, v129\n" \
  "v_mfma_f32_32x32x16_bf16 v[72:87], v[160:163], v[136:139], v[72:87]\n" \
  "v_exp_f32 v130, v130\n" \
  "v_exp_f32 v131, v131\n" \
  "v_exp_f32 v132, v132\n" \
  "v_exp_f32 v133, v133\n" \
  "v_exp_f32 v134, v134\n" \
  "v_exp_f32 v135, v135\n" \
  "v_cvt_pk_bf16_f32 v140, v128, v129\n" \
  "v_cvt_pk_bf16_f32 v141, v130, v131\n" \
  "v_cvt_pk_bf16_f32 v142, v132, v133\n" \
  "v_cvt_pk_bf16_f32 v143, v134, v135\n" \
  "s_nop 0\n" \
  "s_nop 0\n" \
  "v_mfma_f32_32x32x16_bf16 v[72:87], v[164:167], v[140:143], v[72:87]\n" \
  "s_waitcnt lgkmcnt(0)\n" \
  "v_mfma_f32_32x32x16_bf16 v[120:135], v[148:151], v[64:67], v[88:103]\n" \
  "v_add_u32 v70, v70, v71\n" \
  "ds_read_b128 v[160:163], v70\n" \
  "ds_read_b128 v[164:167], v70 offset:512\n" \
  "ds_read_b128 v[144:147], v69 offset:12288\n" \
  "v_exp_f32 v104, v104\n" \
  "v_exp_f32 v105, v105\n" \
  "v_exp_f32 v106, v106\n" \
  "v_exp_f32 v107, v107\n" \
  "v_exp_f32 v108, v108\n" \
  "v_exp_f32 v109, v109\n" \
  "v_exp_f32 v110, v110\n" \
  "v_exp_f32 v111, v111\n" \
  "v_cvt_pk_bf16_f32 v136, v104, v105\n" \
  "v_cvt_pk_bf16_f32 v137, v106, v107\n" \
  "v_cvt_pk_bf16_f32 v138, v108, v109\n" \
  "v_cvt_pk_bf16_f32 v139, v110, v111\n" \
  "v_exp_f32 v112, v112\n" \
  "v_exp_f32 v113, v113\n" \
  "v_mfma_f32_32x32x16_bf16 v[72:87], v[152:155], v[136:139], v[72:87]\n" \
  "v_exp_f32 v114, v114\n" \
  "v_exp_f32 v115, v115\n" \
  "v_exp_f32 v116, v116\n" \
  "v_exp_f32 v117, v117\n" \
  "v_exp_f32 v118, v118\n" \
  "v_exp_f32 v119, v119\n" \
  "v_cvt_pk_bf16_f32 v140, v112, v113\n" \
  "v_cvt_pk_bf16_f32 v141, v114, v115\n" \
  "v_cvt_pk_bf16_f32 v142, v116, v117\n" \
  "v_cvt_pk_bf16_f32 v143, v118, v119\n" \
  "s_nop 0\n" \
  "s_nop 0\n" \
  "v_mfma_f32_32x32x16_bf16 v[72:87], v[156:159], v[140:143], v[72:87]\n" \
  "s_waitcnt lgkmcnt(0)\n" \
  "v_mfma_f32_32x32x16_bf16 v[104:119], v[144:147], v[64:67], v[88:103]\n" \
  "v_add_u32 v70, v70, v71\n" \
  "ds_read_b128 v[152:155], v70\n" \
  "ds_read_b128 v[156:159], v70 offset:512\n" \
  "ds_read_b128 v[148:151], v69 offset:13312\n" \
  "v_exp_f32 v120, v120\n" \
  "v_exp_f32 v121, v121\n" \
  "v_exp_f32 v122, v122\n" \
  "v_exp_f32 v123, v123\n" \
  "v_exp_f32 v124, v124\n" \
  "v_exp_f32 v125, v125\n" \
  "v_exp_f32 v126, v126\n" \
  "v_exp_f32 v127, v127\n" \
  "v_cvt_pk_bf16_f32 v136, v120, v121\n" \
  "v_cvt_pk_bf16_f32 v137, v122, v123\n" \
  "v_cvt_pk_bf16_f32 v138, v124, v125\n" \
  "v_cvt_pk_bf16_f32 v139, v126, v127\n" \
  "v_exp_f32 v128, v128\n" \
  "v_exp_f32 v129, v129\n" \
  "v_mfma_f32_32x32x16_bf16 v[72:87], v[160:163], v[136:139], v[72:87]\n" \
  "v_exp_f32 v130, v130\n" \
  "v_exp_f32 v131, v131\n" \
  "v_exp_f32 v132, v132\n" \
  "v_exp_f32 v133, v133\n" \
  "v_exp_f32 v134, v134\n" \
  "v_exp_f32 v135, v135\n" \
  "v_cvt_pk_bf16_f32 v140, v128, v129\n" \
  "v_cvt_pk_bf16_f32 v141, v130, v131\n" \
  "v_cvt_pk_bf16_f32 v142, v132, v133\n" \
  "v_cvt_pk_bf16_f32 v143, v134, v135\n" \
  "s_nop 0\n" \
  "s_nop 0\n" \
  "v_mfma_f32_32x32x16_bf16 v[72:87], v[164:167], v[140:143], v[72:87]\n" \
  "s_waitcnt lgkmcnt(0)\n" \
  "v_mfma_f32_32x32x16_bf16 v[120:135], v[148:151], v[64:67], v[88:103]\n" \
  "v_add_u32 v70, v70, v71\n" \
  "ds_read_b128 v[160:163], v70\n" \
  "ds_read_b128 v[164:167], v70 offset:512\n" \
  "ds_read_b128 v[144:147], v69 offset:14336\n" \
  "v_exp_f32 v104, v104\n" \
  "v_exp_f32 v105, v105\n" \
  "v_exp_f32 v106, v106\n" \
  "v_exp_f32 v107, v107\n" \
  "v_exp_f32 v108, v108\n" \
  "v_exp_f32 v109, v109\n" \
  "v_exp_f32 v110, v110\n" \
  "v_exp_f32 v111, v111\n" \
  "v_cvt_pk_bf16_f32 v136, v104, v105\n" \
  "v_cvt_pk_bf16_f32 v137, v106, v107\n" \
  "v_cvt_pk_bf16_f32 v138, v108, v109\n" \
  "v_cvt_pk_bf16_f32 v139, v110, v111\n" \
  "v_exp_f32 v112, v112\n" \
  "v_exp_f32 v113, v113\n" \
  "v_mfma_f32_32x32x16_bf16 v[72:87], v[152:155], v[136:139], v[72:87]\n" \
  "v_exp_f32 v114, v114\n" \
  "v_exp_f32 v115, v115\n" \
  "v_exp_f32 v116, v116\n" \
  "v_exp_f32 v117, v117\n" \
  "v_exp_f32 v118, v118\n" \
  "v_exp_f32 v119, v119\n" \
  "v_cvt_pk_bf16_f32 v140, v112, v113\n" \
  "v_cvt_pk_bf16_f32 v141, v114, v115\n" \
  "v_cvt_pk_bf16_f32 v142, v116, v117\n" \
  "v_cvt_pk_bf16_f32 v143, v118, v119\n" \
  "s_nop 0\n" \
  "s_nop 0\n" \
  "v_mfma_f32_32x32x16_bf16 v[72:87], v[156:159], v[140:143], v[72:87]\n" \
  "s_waitcnt lgkmcnt(0)\n" \
  "v_mfma_f32_32x32x16_bf16 v[104:119], v[144:147], v[64:67], v[88:103]\n" \
  "v_add_u32 v70, v70, v71\n" \
  "ds_read_b128 v[152:155], v70\n" \
  "ds_read_b128 v[156:159], v70 offset:512\n" \
  "ds_read_b128 v[148:151], v69 offset:15360\n" \
  "v_exp_f32 v120, v120\n" \
  "v_exp_f32 v121, v121\n" \
  "v_exp_f32 v122, v122\n" \
  "v_exp_f32 v123, v123\n" \
  "v_exp_f32 v124, v124\n" \
  "v_exp_f32 v125, v125\n" \
  "v_exp_f32 v126, v126\n" \
  "v_exp_f32 v127, v127\n" \
  "v_cvt_pk_bf16_f32 v136, v120, v121\n" \
  "v_cvt_pk_bf16_f32 v137, v122, v123\n" \
  "v_cvt_pk_bf16_f32 v138, v124, v125\n" \
  "v_cvt_pk_bf16_f32 v139, v126, v127\n" \
  "v_exp_f32 v128, v128\n" \
  "v_exp_f32 v129, v129\n" \
  "v_mfma_f32_32x32x16_bf16 v[72:87], v[160:163], v[136:139], v[72:87]\n" \
  "v_exp_f32 v130, v130\n" \
  "v_exp_f32 v131, v131\n" \
  "v_exp_f32 v132, v132\n" \
  "v_exp_f32 v133, v133\n" \
  "v_exp_f32 v134, v134\n" \
  "v_exp_f32 v135, v135\n" \
  "v_cvt_pk_bf16_f32 v140, v128, v129\n" \
  "v_cvt_pk_bf16_f32 v141, v130, v131\n" \
  "v_cvt_pk_bf16_f32 v142, v132, v133\n" \
  "v_cvt_pk_bf16_f32 v143, v134, v135\n" \
  "s_nop 0\n" \
  "s_nop 0\n" \
  "v_mfma_f32_32x32x16_bf16 v[72:87], v[164:167], v[140:143], v[72:87]\n" \
  "s_waitcnt lgkmcnt(0)\n" \
  "v_mfma_f32_32x32x16_bf16 v[120:135], v[148:151], v[64:67], v[88:103]\n" \
  "v_add_u32 v70, v70, v71\n" \
  "ds_read_b128 v[160:163], v70\n" \
  "ds_read_b128 v[164:167], v70 offset:512\n" \
  "v_exp_f32 v104, v104\n" \
  "v_exp_f32 v105, v105\n" \
  "v_exp_f32 v106, v106\n" \
  "v_exp_f32 v107, v107\n" \
  "v_exp_f32 v108, v108\n" \
  "v_exp_f32 v109, v109\n" \
  "v_exp_f32 v110, v110\n" \
  "v_exp_f32 v111, v111\n" \
  "v_cvt_pk_bf16_f32 v136, v104, v105\n" \
  "v_cvt_pk_bf16_f32 v137, v106, v107\n" \
  "v_cvt_pk_bf16_f32 v138, v108, v109\n" \
  "v_cvt_pk_bf16_f32 v139, v110, v111\n" \
  "v_exp_f32 v112, v112\n" \
  "v_exp_f32 v113, v113\n" \
  "v_mfma_f32_32x32x16_bf16 v[72:87], v[152:155], v[136:139], v[72:87]\n" \
  "v_exp_f32 v114, v114\n" \
  "v_exp_f32 v115, v115\n" \
  "v_exp_f32 v116, v116\n" \
  "v_exp_f32 v117, v117\n" \
  "v_exp_f32 v118, v118\n" \
  "v_exp_f32 v119, v119\n" \
  "v_cvt_pk_bf16_f32 v140, v112, v113\n" \
  "v_cvt_pk_bf16_f32 v141, v114, v115\n" \
  "v_cvt_pk_bf16_f32 v142, v116, v117\n" \
  "v_cvt_pk_bf16_f32 v143, v118, v119\n" \
  "s_nop 0\n" \
  "s_nop 0\n" \
  "v_mfma_f32_32x32x16_bf16 v[72:87], v[156:159], v[140:143], v[72:87]\n" \
  "v_exp_f32 v120, v120\n" \
  "v_exp_f32 v121, v121\n" \
  "v_exp_f32 v122, v122\n" \
  "v_exp_f32 v123, v123\n" \
  "v_exp_f32 v124, v124\n" \
  "v_exp_f32 v125, v125\n" \
  "v_exp_f32 v126, v126\n" \
  "v_exp_f32 v127, v127\n" \
  "v_cvt_pk_bf16_f32 v136, v120, v121\n" \
  "v_cvt_pk_bf16_f32 v137, v122, v123\n" \
  "v_cvt_pk_bf16_f32 v138, v124, v125\n" \
  "v_cvt_pk_bf16_f32 v139, v126, v127\n" \
  "v_exp_f32 v128, v128\n" \
  "v_exp_f32 v129, v129\n" \
  "s_waitcnt lgkmcnt(1)\n" \
  "v_mfma_f32_32x32x16_bf16 v[72:87], v[160:163], v[136:139], v[72:87]\n" \
  "v_exp_f32 v130, v130\n" \
  "v_exp_f32 v131, v131\n" \
  "v_exp_f32 v132, v132\n" \
  "v_exp_f32 v133, v133\n" \
  "v_exp_f32 v134, v134\n" \
  "v_exp_f32 v135, v135\n" \
  "v_cvt_pk_bf16_f32 v140, v128, v129\n" \
  "v_cvt_pk_bf16_f32 v141, v130, v131\n" \
  "v_cvt_pk_bf16_f32 v142, v132, v133\n" \
  "v_cvt_pk_bf16_f32 v143, v134, v135\n" \
  "s_waitcnt lgkmcnt(0)\n" \
  "s_nop 0\n" \
  "v_mfma_f32_32x32x16_bf16 v[72:87], v[164:167], v[140:143], v[72:87]\n" \
  "s_nop 7\n" \
  "s_nop 3\n" \
  "s_waitcnt lgkmcnt(0)\n" \
  ""
#define ATT_ASM_FIRST \
  "s_nop 1\n" \
  "s_mov_b32 s49, m0\n" \
  "s_add_u32 m0, s46, 0\n" \
  "s_add_u32 s48, s44, 0\n" \
  "buffer_load_dwordx4 v63, s[40:43], s48 offen sc1 lds\n" \
  "s_add_u32 m0, s47, 0\n" \
  "s_add_u32 s48, s45, 0\n" \
  "buffer_load_dwordx4 v63, s[40:43], s48 offen sc1 lds\n" \
  "s_add_u32 m0, s46, 1024\n" \
  "s_add_u32 s48, s44, 1024\n" \
  "buffer_load_dwordx4 v63, s[40:43], s48 offen sc1 lds\n" \
  "s_add_u32 m0, s47, 1024\n" \
  "s_add_u32 s48, s45, 1024\n" \
  "buffer_load_dwordx4 v63, s[40:43], s48 offen sc1 lds\n" \
  "s_add_u32 m0, s46, 2048\n" \
  "s_add_u32 s48, s44, 2048\n" \
  "buffer_load_dwordx4 v63, s[40:43], s48 offen sc1 lds\n" \
  "s_add_u32 m0, s47, 2048\n" \
  "s_add_u32 s48, s45, 2048\n" \
  "buffer_load_dwordx4 v63, s[40:43], s48 offen sc1 lds\n" \
  "s_add_u32 m0, s46, 3072\n" \
  "s_add_u32 s48, s44, 3072\n" \
  "buffer_load_dwordx4 v63, s[40:43], s48 offen sc1 lds\n" \
  "s_add_u32 m0, s47, 3072\n" \
  "s_add_u32 s48, s45, 3072\n" \
  "buffer_load_dwordx4 v63, s[40:43], s48 offen sc1 lds\n" \
  "s_add_u32 m0, s46, 4096\n" \
  "s_add_u32 s48, s44, 4096\n" \
  "buffer_load_dwordx4 v63, s[40:43], s48 offen sc1 lds\n" \
  "s_add_u32 m0, s47, 4096\n" \
  "s_add_u32 s48, s45, 4096\n" \
  "buffer_load_dwordx4 v63, s[40:43], s48 offen sc1 lds\n" \
  "v_mov_b32 v88, v68\n" \
  "v_mov_b32 v89, v68\n" \
  "v_mov_b32 v90, v68\n" \
  "v_mov_b32 v91, v68\n" \
  "v_mov_b32 v92, v68\n" \
  "v_mov_b32 v93, v68\n" \
  "v_mov_b32 v94, v68\n" \
  "v_mov_b32 v95, v68\n" \
  "v_mov_b32 v96, v68\n" \
  "v_mov_b32 v97, v68\n" \
  "v_mov_b32 v98, v68\n" \
  "v_mov_b32 v99, v68\n" \
  "v_mov_b32 v100, v68\n" \
  "v_mov_b32 v101, v68\n" \
  "v_mov_b32 v102, v68\n" \
  "v_mov_b32 v103, v68\n" \
  "s_waitcnt vmcnt(6)\n" \
  "ds_read_b128 v[144:147], v69\n" \
  "ds_read_b128 v[148:151], v69 offset:1024\n" \
  "ds_read_b128 v[152:155], v70\n" \
  "ds_read_b128 v[156:159], v70 offset:512\n" \
  "v_mov_b64 v[72:73], 0\n" \
  "v_mov_b64 v[74:75], 0\n" \
  "v_mov_b64 v[76:77], 0\n" \
  "v_mov_b64 v[78:79], 0\n" \
  "v_mov_b64 v[80:81], 0\n" \
  "v_mov_b64 v[82:83], 0\n" \
  "v_mov_b64 v[84:85], 0\n" \
  "v_mov_b64 v[86:87], 0\n" \
  "s_waitcnt lgkmcnt(3)\n" \
  "v_mfma_f32_32x32x16_bf16 v[104:119], v[144:147], v[64:67], v[88:103]\n" \
  "s_waitcnt lgkmcnt(2)\n" \
  "v_mfma_f32_32x32x16_bf16 v[120:135], v[148:151], v[64:67], v[88:103]\n" \
  "s_add_u32 m0, s46, 5120\n" \
  "s_add_u32 s48, s44, 5120\n" \
  "buffer_load_dwordx4 v63, s[40:43], s48 offen sc1 lds\n" \
  "s_add_u32 m0, s47, 5120\n" \
  "s_add_u32 s48, s45, 5120\n" \
  "buffer_load_dwordx4 v63, s[40:43], s48 offen sc1 lds\n" \
  "s_waitcnt vmcnt(7)\n" \
  "v_add_u32 v70, v70, v71\n" \
  "ds_read_b128 v[160:163], v70\n" \
  "ds_read_b128 v[164:167], v70 offset:512\n" \
  "ds_read_b128 v[144:147], v69 offset:2048\n" \
  "v_exp_f32 v104, v104\n" \
  "v_exp_f32 v105, v105\n" \
  "v_exp_f32 v106, v106\n" \
  "v_exp_f32 v107, v107\n" \
  "v_exp_f32 v108, v108\n" \
  "v_exp_f32 v109, v109\n" \
  "v_exp_f32 v110, v110\n" \
  "v_exp_f32 v111, v111\n" \
  "v_cvt_pk_bf16_f32 v136, v104, v105\n" \
  "v_cvt_pk_bf16_f32 v137, v106, v107\n" \
  "v_cvt_pk_bf16_f32 v138, v108, v109\n" \
  "v_cvt_pk_bf16_f32 v139, v110, v111\n" \
  "v_exp_f32 v112, v112\n" \
  "v_exp_f32 v113, v113\n" \
  "s_waitcnt lgkmcnt(4)\n" \
  "v_mfma_f32_32x32x16_bf16 v[72:87], v[152:155], v[136:139], v[72:87]\n" \
  "v_exp_f32 v114, v114\n" \
  "v_exp_f32 v115, v115\n" \
  "v_exp_f32 v116, v116\n" \
  "v_exp_f32 v117, v117\n" \
  "v_exp_f32 v118, v118\n" \
  "v_exp_f32 v119, v119\n" \
  "v_cvt_pk_bf16_f32 v140, v112, v113\n" \
  "v_cvt_pk_bf16_f32 v141, v114, v115\n" \
  "v_cvt_pk_bf16_f32 v142, v116, v117\n" \
  "v_cvt_pk_bf16_f32 v143, v118, v119\n" \
  "s_waitcnt lgkmcnt(3)\n" \
  "s_nop 0\n" \
  "v_mfma_f32_32x32x16_bf16 v[72:87], v[156:159], v[140:143], v[72:87]\n" \
  "s_waitcnt lgkmcnt(0)\n" \
  "v_mfma_f32_32x32x16_bf16 v[104:119], v[144:147], v[64:67], v[88:103]\n" \
  "s_add_u32 m0, s46, 6144\n" \
  "s_add_u32 s48, s44, 6144\n" \
  "buffer_load_dwordx4 v63, s[40:43], s48 offen sc1 lds\n" \
  "s_add_u32 m0, s47, 6144\n" \
  "s_add_u32 s48, s45, 6144\n" \
  "buffer_load_dwordx4 v63, s[40:43], s48 offen sc1 lds\n" \
  "s_waitcnt vmcnt(7)\n" \
  "v_add_u32 v70, v70, v71\n" \
  "ds_read_b128 v[152:155], v70\n" \
  "ds_read_b128 v[156:159], v70 offset:512\n" \
  "ds_read_b128 v[148:151], v69 offset:3072\n" \
  "v_exp_f32 v120, v120\n" \
  "v_exp_f32 v121, v121\n" \
  "v_exp_f32 v122, v122\n" \
  "v_exp_f32 v123, v123\n" \
  "v_exp_f32 v124, v124\n" \
  "v_exp_f32 v125, v125\n" \
  "v_exp_f32 v126, v126\n" \
  "v_exp_f32 v127, v127\n" \
  "v_cvt_pk_bf16_f32 v136, v120, v121\n" \
  "v_cvt_pk_bf16_f32 v137, v122, v123\n" \
  "v_cvt_pk_bf16_f32 v138, v124, v125\n" \
  "v_cvt_pk_bf16_f32 v139, v126, v127\n" \
  "v_exp_f32 v128, v128\n" \
  "v_exp_f32 v129, v129\n" \
  "v_mfma_f32_32x32x16_bf16 v[72:87], v[160:163], v[136:139], v[72:87]\n" \
  "v_exp_f32 v130, v130\n" \
  "v_exp_f32 v131, v131\n" \
  "v_exp_f32 v132, v132\n" \
  "v_exp_f32 v133, v133\n" \
  "v_exp_f32 v134, v134\n" \
  "v_exp_f32 v135, v135\n" \
  "v_cvt_pk_bf16_f32 v140, v128, v129\n" \
  "v_cvt_pk_bf16_f32 v141, v130, v131\n" \
  "v_cvt_pk_bf16_f32 v142, v132, v133\n" \
  "v_cvt_pk_bf16_f32 v143, v134, v135\n" \
  "s_nop 0\n" \
  "s_nop 0\n" \
  "v_mfma_f32_32x32x16_bf16 v[72:87], v[164:167], v[140:143], v[72:87]\n" \
  "s_waitcnt lgkmcnt(0)\n" \
  "v_mfma_f32_32x32x16_bf16 v[120:135], v[148:151], v[64:67], v[88:103]\n" \
  "s_add_u32 m0, s46, 7168\n" \
  "s_add_u32 s48, s44, 7168\n" \
  "buffer_load_dwordx4 v63, s[40:43], s48 offen sc1 lds\n" \
  "s_add_u32 m0, s47, 7168\n" \
  "s_add_u32 s48, s45, 7168\n" \
  "buffer_load_dwordx4 v63, s[40:43], s48 offen sc1 lds\n" \
  "s_waitcnt vmcnt(7)\n" \
  "v_add_u32 v70, v70, v71\n" \
  "ds_read_b128 v[160:163], v70\n" \
  "ds_read_b128 v[164:167], v70 offset:512\n" \
  "ds_read_b128 v[144:147], v69 offset:4096\n" \
  "v_exp_f32 v104, v104\n" \
  "v_exp_f32 v105, v105\n" \
  "v_exp_f32 v106, v106\n" \
  "v_exp_f32 v107, v107\n" \
  "v_exp_f32 v108, v108\n" \
  "v_exp_f32 v109, v109\n" \
  "v_exp_f32 v110, v110\n" \
  "v_exp_f32 v111, v111\n" \
  "v_cvt_pk_bf16_f32 v136, v104, v105\n" \
  "v_cvt_pk_bf16_f32 v137, v106, v107\n" \
  "v_cvt_pk_bf16_f32 v138, v108, v109\n" \
  "v_cvt_pk_bf16_f32 v139, v110, v111\n" \
  "v_exp_f32 v112, v112\n" \
  "v_exp_f32 v113, v113\n" \
  "v_mfma_f32_32x32x16_bf16 v[72:87], v[152:155], v[136:139], v[72:87]\n" \
  "v_exp_f32 v114, v114\n" \
  "v_exp_f32 v115, v115\n" \
  "v_exp_f32 v116, v116\n" \
  "v_exp_f32 v117, v117\n" \
  "v_exp_f32 v118, v118\n" \
  "v_exp_f32 v119, v119\n" \
  "v_cvt_pk_bf16_f32 v140, v112, v113\n" \
  "v_cvt_pk_bf16_f32 v141, v114, v115\n" \
  "v_cvt_pk_bf16_f32 v142, v116, v117\n" \
  "v_cvt_pk_bf16_f32 v143, v118, v119\n" \
  "s_nop 0\n" \
  "s_nop 0\n" \
  "v_mfma_f32_32x32x16_bf16 v[72:87], v[156:159], v[140:143], v[72:87]\n" \
  "s_waitcnt lgkmcnt(0)\n" \
  "v_mfma_f32_32x32x16_bf16 v[104:119], v[144:147], v[64:67], v[88:103]\n" \
  "s_add_u32 m0, s46, 8192\n" \
  "s_add_u32 s48, s44, 8192\n" \
  "buffer_load_dwordx4 v63, s[40:43], s48 offen sc1 lds\n" \
  "s_add_u32 m0, s47, 8192\n" \
  "s_add_u32 s48, s45, 8192\n" \
  "buffer_load_dwordx4 v63, s[40:43], s48 offen sc1 lds\n" \
  "s_waitcnt vmcnt(7)\n" \
  "v_add_u32 v70, v70, v71\n" \
  "ds_read_b128 v[152:155], v70\n" \
  "ds_read_b128 v[156:159], v70 offset:512\n" \
  "ds_read_b128 v[148:151], v69 offset:5120\n" \
  "v_exp_f32 v120, v120\n" \
  "v_exp_f32 v121, v121\n" \
  "v_exp_f32 v122, v122\n" \
  "v_exp_f32 v123, v123\n" \
  "v_exp_f32 v124, v124\n" \
  "v_exp_f32 v125, v125\n" \
  "v_exp_f32 v126, v126\n" \
  "v_exp_f32 v127, v127\n" \
  "v_cvt_pk_bf16_f32 v136, v120, v121\n" \
  "v_cvt_pk_bf16_f32 v137, v122, v123\n" \
  "v_cvt_pk_bf16_f32 v138, v124, v125\n" \
  "v_cvt_pk_bf16_f32 v139, v126, v127\n" \
  "v_exp_f32 v128, v128\n" \
  "v_exp_f32 v129, v129\n" \
  "v_mfma_f32_32x32x16_bf16 v[72:87], v[160:163], v[136:139], v[72:87]\n" \
  "v_exp_f32 v130, v130\n" \
  "v_exp_f32 v131, v131\n" \
  "v_exp_f32 v132, v132\n" \
  "v_exp_f32 v133, v133\n" \
  "v_exp_f32 v134, v134\n" \
  "v_exp_f32 v135, v135\n" \
  "v_cvt_pk_bf16_f32 v140, v128, v129\n" \
  "v_cvt_pk_bf16_f32 v141, v130, v131\n" \
  "v_cvt_pk_bf16_f32 v142, v132, v133\n" \
  "v_cvt_pk_bf16_f32 v143, v134, v135\n" \
  "s_nop 0\n" \
  "s_nop 0\n" \
  "v_mfma_f32_32x32x16_bf16 v[72:87], v[164:167], v[140:143], v[72:87]\n" \
  "s_waitcnt lgkmcnt(0)\n" \
  "v_mfma_f32_32x32x16_bf16 v[120:135], v[148:151], v[64:67], v[88:103]\n" \
  "s_add_u32 m0, s46, 9216\n" \
  "s_add_u32 s48, s44, 9216\n" \
  "buffer_load_dwordx4 v63, s[40:43], s48 offen sc1 lds\n" \
  "s_add_u32 m0, s47, 9216\n" \
  "s_add_u32 s48, s45, 9216\n" \
  "buffer_load_dwordx4 v63, s[40:43], s48 offen sc1 lds\n" \
  "s_waitcnt vmcnt(7)\n" \
  "v_add_u32 v70, v70, v71\n" \
  "ds_read_b128 v[160:163], v70\n" \
  "ds_read_b128 v[164:167], v70 offset:512\n" \
  "ds_read_b128 v[144:147], v69 offset:6144\n" \
  "v_exp_f32 v104, v104\n" \
  "v_exp_f32 v105, v105\n" \
  "v_exp_f32 v106, v106\n" \
  "v_exp_f32 v107, v107\n" \
  "v_exp_f32 v108, v108\n" \
  "v_exp_f32 v109, v109\n" \
  "v_exp_f32 v110, v110\n" \
  "v_exp_f32 v111, v111\n" \
  "v_cvt_pk_bf16_f32 v136, v104, v105\n" \
  "v_cvt_pk_bf16_f32 v137, v106, v107\n" \
  "v_cvt_pk_bf16_f32 v138, v108, v109\n" \
  "v_cvt_pk_bf16_f32 v139, v110, v111\n" \
  "v_exp_f32 v112, v112\n" \
  "v_exp_f32 v113, v113\n" \
  "v_mfma_f32_32x32x16_bf16 v[72:87], v[152:155], v[136:139], v[72:87]\n" \
  "v_exp_f32 v114, v114\n" \
  "v_exp_f32 v115, v115\n" \
  "v_exp_f32 v116, v116\n" \
  "v_exp_f32 v117, v117\n" \
  "v_exp_f32 v118, v118\n" \
  "v_exp_f32 v119, v119\n" \
  "v_cvt_pk_bf16_f32 v140, v112, v113\n" \
  "v_cvt_pk_bf16_f32 v141, v114, v115\n" \
  "v_cvt_pk_bf16_f32 v142, v116, v117\n" \
  "v_cvt_pk_bf16_f32 v143, v118, v119\n" \
  "s_nop 0\n" \
  "s_nop 0\n" \
  "v_mfma_f32_32x32x16_bf16 v[72:87], v[156:159], v[140:143], v[72:87]\n" \
  "s_waitcnt lgkmcnt(0)\n" \
  "v_mfma_f32_32x32x16_bf16 v[104:119], v[144:147], v[64:67], v[88:103]\n" \
  "s_add_u32 m0, s46, 10240\n" \
  "s_add_u32 s48, s44, 10240\n" \
  "buffer_load_dwordx4 v63, s[40:43], s48 offen sc1 lds\n" \
  "s_add_u32 m0, s47, 10240\n" \
  "s_add_u32 s48, s45, 10240\n" \
  "buffer_load_dwordx4 v63, s[40:43], s48 offen sc1 lds\n" \
  "s_waitcnt vmcnt(7)\n" \
  "v_add_u32 v70, v70, v71\n" \
  "ds_read_b128 v[152:155], v70\n" \
  "ds_read_b128 v[156:159], v70 offset:512\n" \
  "ds_read_b128 v[148:151], v69 offset:7168\n" \
  "v_exp_f32 v120, v120\n" \
  "v_exp_f32 v121, v121\n" \
  "v_exp_f32 v122, v122\n" \
  "v_exp_f32 v123, v123\n" \
  "v_exp_f32 v124, v124\n" \
  "v_exp_f32 v125, v125\n" \
  "v_exp_f32 v126, v126\n" \
  "v_exp_f32 v127, v127\n" \
  "v_cvt_pk_bf16_f32 v136, v120, v121\n" \
  "v_cvt_pk_bf16_f32 v137, v122, v123\n" \
  "v_cvt_pk_bf16_f32 v138, v124, v125\n" \
  "v_cvt_pk_bf16_f32 v139, v126, v127\n" \
  "v_exp_f32 v128, v128\n" \
  "v_exp_f32 v129, v129\n" \
  "v_mfma_f32_32x32x16_bf16 v[72:87], v[160:163], v[136:139], v[72:87]\n" \
  "v_exp_f32 v130, v130\n" \
  "v_exp_f32 v131, v131\n" \
  "v_exp_f32 v132, v132\n" \
  "v_exp_f32 v133, v133\n" \
  "v_exp_f32 v134, v134\n" \
  "v_exp_f32 v135, v135\n" \
  "v_cvt_pk_bf16_f32 v140, v128, v129\n" \
  "v_cvt_pk_bf16_f32 v141, v130, v131\n" \
  "v_cvt_pk_bf16_f32 v142, v132, v133\n" \
  "v_cvt_pk_bf16_f32 v143, v134, v135\n" \
  "s_nop 0\n" \
  "s_nop 0\n" \
  "v_mfma_f32_32x32x16_bf16 v[72:87], v[164:167], v[140:143], v[72:87]\n" \
  "s_waitcnt lgkmcnt(0)\n" \
  "v_mfma_f32_32x32x16_bf16 v[120:135], v[148:151], v[64:67], v[88:103]\n" \
  "s_add_u32 m0, s46, 11264\n" \
  "s_add_u32 s48, s44, 11264\n" \
  "buffer_load_dwordx4 v63, s[40:43], s48 offen sc1 lds\n" \
  "s_add_u32 m0, s47, 11264\n" \
  "s_add_u32 s48, s45, 11264\n" \
  "buffer_load_dwordx4 v63, s[40:43], s48 offen sc1 lds\n" \
  "s_waitcnt vmcnt(7)\n" \
  "v_add_u32 v70, v70, v71\n" \
  "ds_read_b128 v[160:163], v70\n" \
  "ds_read_b128 v[164:167], v70 offset:512\n" \
  "ds_read_b128 v[144:147], v69 offset:8192\n" \
  "v_exp_f32 v104, v104\n" \
  "v_exp_f32 v105, v105\n" \
  "v_exp_f32 v106, v106\n" \
  "v_exp_f32 v107, v107\n" \
  "v_exp_f32 v108, v108\n" \
  "v_exp_f32 v109, v109\n" \
  "v_exp_f32 v110, v110\n" \
  "v_exp_f32 v111, v111\n" \
  "v_cvt_pk_bf16_f32 v136, v104, v105\n" \
  "v_cvt_pk_bf16_f32 v137, v106, v107\n" \
  "v_cvt_pk_bf16_f32 v138, v108, v109\n" \
  "v_cvt_pk_bf16_f32 v139, v110, v111\n" \
  "v_exp_f32 v112, v112\n" \
  "v_exp_f32 v113, v113\n" \
  "v_mfma_f32_32x32x16_bf16 v[72:87], v[152:155], v[136:139], v[72:87]\n" \
  "v_exp_f32 v114, v114\n" \
  "v_exp_f32 v115, v115\n" \
  "v_exp_f32 v116, v116\n" \
  "v_exp_f32 v117, v117\n" \
  "v_exp_f32 v118, v118\n" \
  "v_exp_f32 v119, v119\n" \
  "v_cvt_pk_bf16_f32 v140, v112, v113\n" \
  "v_cvt_pk_bf16_f32 v141, v114, v115\n" \
  "v_cvt_pk_bf16_f32 v142, v116, v117\n" \
  "v_cvt_pk_bf16_f32 v143, v118, v119\n" \
  "s_nop 0\n" \
  "s_nop 0\n" \
  "v_mfma_f32_32x32x16_bf16 v[72:87], v[156:159], v[140:143], v[72:87]\n" \
  "s_waitcnt lgkmcnt(0)\n" \
  "v_mfma_f32_32x32x16_bf16 v[104:119], v[144:147], v[64:67], v[88:103]\n" \
  "s_add_u32 m0, s46, 12288\n" \
  "s_add_u32 s48, s44, 12288\n" \
  "buffer_load_dwordx4 v63, s[40:43], s48 offen sc1 lds\n" \
  "s_add_u32 m0, s47, 12288\n" \
  "s_add_u32 s48, s45, 12288\n" \
  "buffer_load_dwordx4 v63, s[40:43], s48 offen sc1 lds\n" \
  "s_waitcnt vmcnt(7)\n" \
  "v_add_u32 v70, v70, v71\n" \
  "ds_read_b128 v[152:155], v70\n" \
  "ds_read_b128 v[156:159], v70 offset:512\n" \
  "ds_read_b128 v[148:151], v69 offset:9216\n" \
  "v_exp_f32 v120, v120\n" \
  "v_exp_f32 v121, v121\n" \
  "v_exp_f32 v122, v122\n" \
  "v_exp_f32 v123, v123\n" \
  "v_exp_f32 v124, v124\n" \
  "v_exp_f32 v125, v125\n" \
  "v_exp_f32 v126, v126\n" \
  "v_exp_f32 v127, v127\n" \
  "v_cvt_pk_bf16_f32 v136, v120, v121\n" \
  "v_cvt_pk_bf16_f32 v137, v122, v123\n" \
  "v_cvt_pk_bf16_f32 v138, v124, v125\n" \
  "v_cvt_pk_bf16_f32 v139, v126, v127\n" \
  "v_exp_f32 v128, v128\n" \
  "v_exp_f32 v129, v129\n" \
  "v_mfma_f32_32x32x16_bf16 v[72:87], v[160:163], v[136:139], v[72:87]\n" \
  "v_exp_f32 v130, v130\n" \
  "v_exp_f32 v131, v131\n" \
  "v_exp_f32 v132, v132\n" \
  "v_exp_f32 v133, v133\n" \
  "v_exp_f32 v134, v134\n" \
  "v_exp_f32 v135, v135\n" \
  "v_cvt_pk_bf16_f32 v140, v128, v129\n" \
  "v_cvt_pk_bf16_f32 v141, v130, v131\n" \
  "v_cvt_pk_bf16_f32 v142, v132, v133\n" \
  "v_cvt_pk_bf16_f32 v143, v134, v135\n" \
  "s_nop 0\n" \
  "s_nop 0\n" \
  "v_mfma_f32_32x32x16_bf16 v[72:87], v[164:167], v[140:143], v[72:87]\n" \
  "s_waitcnt lgkmcnt(0)\n" \
  "v_mfma_f32_32x32x16_bf16 v[120:135], v[148:151], v[64:67], v[88:103]\n" \
  "s_add_u32 m0, s46, 13312\n" \
  "s_add_u32 s48, s44, 13312\n" \
  "buffer_load_dwordx4 v63, s[40:43], s48 offen sc1 lds\n" \
  "s_add_u32 m0, s47, 13312\n" \
  "s_add_u32 s48, s45, 13312\n" \
  "buffer_load_dwordx4 v63, s[40:43], s48 offen sc1 lds\n" \
  "s_waitcnt vmcnt(7)\n" \
  "v_add_u32 v70, v70, v71\n" \
  "ds_read_b128 v[160:163], v70\n" \
  "ds_read_b128 v[164:167], v70 offset:512\n" \
  "ds_read_b128 v[144:147], v69 offset:10240\n" \
  "v_exp_f32 v104, v104\n" \
  "v_exp_f32 v105, v105\n" \
  "v_exp_f32 v106, v106\n" \
  "v_exp_f32 v107, v107\n" \
  "v_exp_f32 v108, v108\n" \
  "v_exp_f32 v109, v109\n" \
  "v_exp_f32 v110, v110\n" \
  "v_exp_f32 v111, v111\n" \
  "v_cvt_pk_bf16_f32 v136, v104, v105\n" \
  "v_cvt_pk_bf16_f32 v137, v106, v107\n" \
  "v_cvt_pk_bf16_f32 v138, v108, v109\n" \
  "v_cvt_pk_bf16_f32 v139, v110, v111\n" \
  "v_exp_f32 v112, v112\n" \
  "v_exp_f32 v113, v113\n" \
  "v_mfma_f32_32x32x16_bf16 v[72:87], v[152:155], v[136:139], v[72:87]\n" \
  "v_exp_f32 v114, v114\n" \
  "v_exp_f32 v115, v115\n" \
  "v_exp_f32 v116, v116\n" \
  "v_exp_f32 v117, v117\n" \
  "v_exp_f32 v118, v118\n" \
  "v_exp_f32 v119, v119\n" \
  "v_cvt_pk_bf16_f32 v140, v112, v113\n" \
  "v_cvt_pk_bf16_f32 v141, v114, v115\n" \
  "v_cvt_pk_bf16_f32 v142, v116, v117\n" \
  "v_cvt_pk_bf16_f32 v143, v118, v119\n" \
  "s_nop 0\n" \
  "s_nop 0\n" \
  "v_mfma_f32_32x32x16_bf16 v[72:87], v[156:159], v[140:143], v[72:87]\n" \
  "s_waitcnt lgkmcnt(0)\n" \
  "v_mfma_f32_32x32x16_bf16 v[104:119], v[144:147], v[64:67], v[88:103]\n" \
  "s_add_u32 m0, s46, 14336\n" \
  "s_add_u32 s48, s44, 14336\n" \
  "buffer_load_dwordx4 v63, s[40:43], s48 offen sc1 lds\n" \
  "s_add_u32 m0, s47, 14336\n" \
  "s_add_u32 s48, s45, 14336\n" \
  "buffer_load_dwordx4 v63, s[40:43], s48 offen sc1 lds\n" \
  "s_waitcnt vmcnt(7)\n" \
  "v_add_u32 v70, v70, v71\n" \
  "ds_read_b128 v[152:155], v70\n" \
  "ds_read_b128 v[156:159], v70 offset:512\n" \
  "ds_read_b128 v[148:151], v69 offset:11264\n" \
  "v_exp_f32 v120, v120\n" \
  "v_exp_f32 v121, v121\n" \
  "v_exp_f32 v122, v122\n" \
  "v_exp_f32 v123, v123\n" \
  "v_exp_f32 v124, v124\n" \
  "v_exp_f32 v125, v125\n" \
  "v_exp_f32 v126, v126\n" \
  "v_exp_f32 v127, v127\n" \
  "v_cvt_pk_bf16_f32 v136, v120, v121\n" \
  "v_cvt_pk_bf16_f32 v137, v122, v123\n" \
  "v_cvt_pk_bf16_f32 v138, v124, v125\n" \
  "v_cvt_pk_bf16_f32 v139, v126, v127\n" \
  "v_exp_f32 v128, v128\n" \
  "v_exp_f32 v129, v129\n" \
  "v_mfma_f32_32x32x16_bf16 v[72:87], v[160:163], v[136:139], v[72:87]\n" \
  "v_exp_f32 v130, v130\n" \
  "v_exp_f32 v131, v131\n" \
  "v_exp_f32 v132, v132\n" \
  "v_exp_f32 v133, v133\n" \
  "v_exp_f32 v134, v134\n" \
  "v_exp_f32 v135, v135\n" \
  "v_cvt_pk_bf16_f32 v140, v128, v129\n" \
  "v_cvt_pk_bf16_f32 v141, v130, v131\n" \
  "v_cvt_pk_bf16_f32 v142, v132, v133\n" \
  "v_cvt_pk_bf16_f32 v143, v134, v135\n" \
  "s_nop 0\n" \
  "s_nop 0\n" \
  "v_mfma_f32_32x32x16_bf16 v[72:87], v[164:167], v[140:143], v[72:87]\n" \
  "s_waitcnt lgkmcnt(0)\n" \
  "v_mfma_f32_32x32x16_bf16 v[120:135], v[148:151], v[64:67], v[88:103]\n" \
  "s_add_u32 m0, s46, 15360\n" \
  "s_add_u32 s48, s44, 15360\n" \
  "buffer_load_dwordx4 v63, s[40:43], s48 offen sc1 lds\n" \
  "s_add_u32 m0, s47, 15360\n" \
  "s_add_u32 s48, s45, 15360\n" \
  "buffer_load_dwordx4 v63, s[40:43], s48 offen sc1 lds\n" \
  "s_waitcnt vmcnt(7)\n" \
  "v_add_u32 v70, v70, v71\n" \
  "ds_read_b128 v[160:163], v70\n" \
  "ds_read_b128 v[164:167], v70 offset:512\n" \
  "ds_read_b128 v[144:147], v69 offset:12288\n" \
  "v_exp_f32 v104, v104\n" \
  "v_exp_f32 v105, v105\n" \
  "v_exp_f32 v106, v106\n" \
  "v_exp_f32 v107, v107\n" \
  "v_exp_f32 v108, v108\n" \
  "v_exp_f32 v109, v109\n" \
  "v_exp_f32 v110, v110\n" \
  "v_exp_f32 v111, v111\n" \
  "v_cvt_pk_bf16_f32 v136, v104, v105\n" \
  "v_cvt_pk_bf16_f32 v137, v106, v107\n" \
  "v_cvt_pk_bf16_f32 v138, v108, v109\n" \
  "v_cvt_pk_bf16_f32 v139, v110, v111\n" \
  "v_exp_f32 v112, v112\n" \
  "v_exp_f32 v113, v113\n" \
  "v_mfma_f32_32x32x16_bf16 v[72:87], v[152:155], v[136:139], v[72:87]\n" \
  "v_exp_f32 v114, v114\n" \
  "v_exp_f32 v115, v115\n" \
  "v_exp_f32 v116, v116\n" \
  "v_exp_f32 v117, v117\n" \
  "v_exp_f32 v118, v118\n" \
  "v_exp_f32 v119, v119\n" \
  "v_cvt_pk_bf16_f32 v140, v112, v113\n" \
  "v_cvt_pk_bf16_f32 v141, v114, v115\n" \
  "v_cvt_pk_bf16_f32 v142, v116, v117\n" \
  "v_cvt_pk_bf16_f32 v143, v118, v119\n" \
  "s_nop 0\n" \
  "s_nop 0\n" \
  "v_mfma_f32_32x32x16_bf16 v[72:87], v[156:159], v[140:143], v[72:87]\n" \
  "s_waitcnt lgkmcnt(0)\n" \
  "v_mfma_f32_32x32x16_bf16 v[104:119], v[144:147], v[64:67], v[88:103]\n" \
  "s_waitcnt vmcnt(5)\n" \
  "v_add_u32 v70, v70, v71\n" \
  "ds_read_b128 v[152:155], v70\n" \
  "ds_read_b128 v[156:159], v70 offset:512\n" \
  "ds_read_b128 v[148:151], v69 offset:13312\n" \
  "v_exp_f32 v120, v120\n" \
  "v_exp_f32 v121, v121\n" \
  "v_exp_f32 v122, v122\n" \
  "v_exp_f32 v123, v123\n" \
  "v_exp_f32 v124, v124\n" \
  "v_exp_f32 v125, v125\n" \
  "v_exp_f32 v126, v126\n" \
  "v_exp_f32 v127, v127\n" \
  "v_cvt_pk_bf16_f32 v136, v120, v121\n" \
  "v_cvt_pk_bf16_f32 v137, v122, v123\n" \
  "v_cvt_pk_bf16_f32 v138, v124, v125\n" \
  "v_cvt_pk_bf16_f32 v139, v126, v127\n" \
  "v_exp_f32 v128, v128\n" \
  "v_exp_f32 v129, v129\n" \
  "v_mfma_f32_32x32x16_bf16 v[72:87], v[160:163], v[136:139], v[72:87]\n" \
  "v_exp_f32 v130, v130\n" \
  "v_exp_f32 v131, v131\n" \
  "v_exp_f32 v132, v132\n" \
  "v_exp_f32 v133, v133\n" \
  "v_exp_f32 v134, v134\n" \
  "v_exp_f32 v135, v135\n" \
  "v_cvt_pk_bf16_f32 v140, v128, v129\n" \
  "v_cvt_pk_bf16_f32 v141, v130, v131\n" \
  "v_cvt_pk_bf16_f32 v142, v132, v133\n" \
  "v_cvt_pk_bf16_f32 v143, v134, v135\n" \
  "s_nop 0\n" \
  "s_nop 0\n" \
  "v_mfma_f32_32x32x16_bf16 v[72:87], v[164:167], v[140:143], v[72:87]\n" \
  "s_waitcnt lgkmcnt(0)\n" \
  "v_mfma_f32_32x32x16_bf16 v[120:135], v[148:151], v[64:67], v[88:103]\n" \
  "s_waitcnt vmcnt(3)\n" \
  "v_add_u32 v70, v70, v71\n" \
  "ds_read_b128 v[160:163], v70\n" \
  "ds_read_b128 v[164:167], v70 offset:512\n" \
  "ds_read_b128 v[144:147], v69 offset:14336\n" \
  "v_exp_f32 v104, v104\n" \
  "v_exp_f32 v105, v105\n" \
  "v_exp_f32 v106, v106\n" \
  "v_exp_f32 v107, v107\n" \
  "v_exp_f32 v108, v108\n" \
  "v_exp_f32 v109, v109\n" \
  "v_exp_f32 v110, v110\n" \
  "v_exp_f32 v111, v111\n" \
  "v_cvt_pk_bf16_f32 v136, v104, v105\n" \
  "v_cvt_pk_bf16_f32 v137, v106, v107\n" \
  "v_cvt_pk_bf16_f32 v138, v108, v109\n" \
  "v_cvt_pk_bf16_f32 v139, v110, v111\n" \
  "v_exp_f32 v112, v112\n" \
  "v_exp_f32 v113, v113\n" \
  "v_mfma_f32_32x32x16_bf16 v[72:87], v[152:155], v[136:139], v[72:87]\n" \
  "v_exp_f32 v114, v114\n" \
  "v_exp_f32 v115, v115\n" \
  "v_exp_f32 v116, v116\n" \
  "v_exp_f32 v117, v117\n" \
  "v_exp_f32 v118, v118\n" \
  "v_exp_f32 v119, v119\n" \
  "v_cvt_pk_bf16_f32 v140, v112, v113\n" \
  "v_cvt_pk_bf16_f32 v141, v114, v115\n" \
  "v_cvt_pk_bf16_f32 v142, v116, v117\n" \
  "v_cvt_pk_bf16_f32 v143, v118, v119\n" \
  "s_nop 0\n" \
  "s_nop 0\n" \
  "v_mfma_f32_32x32x16_bf16 v[72:87], v[156:159], v[140:143], v[72:87]\n" \
  "s_waitcnt lgkmcnt(0)\n" \
  "v_mfma_f32_32x32x16_bf16 v[104:119], v[144:147], v[64:67], v[88:103]\n" \
  "s_waitcnt vmcnt(1)\n" \
  "v_add_u32 v70, v70, v71\n" \
  "ds_read_b128 v[152:155], v70\n" \
  "ds_read_b128 v[156:159], v70 offset:512\n" \
  "ds_read_b128 v[148:151], v69 offset:15360\n" \
  "v_exp_f32 v120, v120\n" \
  "v_exp_f32 v121, v121\n" \
  "v_exp_f32 v122, v122\n" \
  "v_exp_f32 v123, v123\n" \
  "v_exp_f32 v124, v124\n" \
  "v_exp_f32 v125, v125\n" \
  "v_exp_f32 v126, v126\n" \
  "v_exp_f32 v127, v127\n" \
  "v_cvt_pk_bf16_f32 v136, v120, v121\n" \
  "v_cvt_pk_bf16_f32 v137, v122, v123\n" \
  "v_cvt_pk_bf16_f32 v138, v124, v125\n" \
  "v_cvt_pk_bf16_f32 v139, v126, v127\n" \
  "v_exp_f32 v128, v128\n" \
  "v_exp_f32 v129, v129\n" \
  "v_mfma_f32_32x32x16_bf16 v[72:87], v[160:163], v[136:139], v[72:87]\n" \
  "v_exp_f32 v130, v130\n" \
  "v_exp_f32 v131, v131\n" \
  "v_exp_f32 v132, v132\n" \
  "v_exp_f32 v133, v133\n" \
  "v_exp_f32 v134, v134\n" \
  "v_exp_f32 v135, v135\n" \
  "v_cvt_pk_bf16_f32 v140, v128, v129\n" \
  "v_cvt_pk_bf16_f32 v141, v130, v131\n" \
  "v_cvt_pk_bf16_f32 v142, v132, v133\n" \
  "v_cvt_pk_bf16_f32 v143, v134, v135\n" \
  "s_nop 0\n" \
  "s_nop 0\n" \
  "v_mfma_f32_32x32x16_bf16 v[72:87], v[164:167], v[140:143], v[72:87]\n" \
  "s_waitcnt lgkmcnt(0)\n" \
  "v_mfma_f32_32x32x16_bf16 v[120:135], v[148:151], v[64:67], v[88:103]\n" \
  "s_waitcnt vmcnt(0)\n" \
  "v_add_u32 v70, v70, v71\n" \
  "ds_read_b128 v[160:163], v70\n" \
  "ds_read_b128 v[164:167], v70 offset:512\n" \
  "v_exp_f32 v104, v104\n" \
  "v_exp_f32 v105, v105\n" \
  "v_exp_f32 v106, v106\n" \
  "v_exp_f32 v107, v107\n" \
  "v_exp_f32 v108, v108\n" \
  "v_exp_f32 v109, v109\n" \
  "v_exp_f32 v110, v110\n" \
  "v_exp_f32 v111, v111\n" \
  "v_cvt_pk_bf16_f32 v136, v104, v105\n" \
  "v_cvt_pk_bf16_f32 v137, v106, v107\n" \
  "v_cvt_pk_bf16_f32 v138, v108, v109\n" \
  "v_cvt_pk_bf16_f32 v139, v110, v111\n" \
  "v_exp_f32 v112, v112\n" \
  "v_exp_f32 v113, v113\n" \
  "v_mfma_f32_32x32x16_bf16 v[72:87], v[152:155], v[136:139], v[72:87]\n" \
  "v_exp_f32 v114, v114\n" \
  "v_exp_f32 v115, v115\n" \
  "v_exp_f32 v116, v116\n" \
  "v_exp_f32 v117, v117\n" \
  "v_exp_f32 v118, v118\n" \
  "v_exp_f32 v119, v119\n" \
  "v_cvt_pk_bf16_f32 v140, v112, v113\n" \
  "v_cvt_pk_bf16_f32 v141, v114, v115\n" \
  "v_cvt_pk_bf16_f32 v142, v116, v117\n" \
  "v_cvt_pk_bf16_f32 v143, v118, v119\n" \
  "s_nop 0\n" \
  "s_nop 0\n" \
  "v_mfma_f32_32x32x16_bf16 v[72:87], v[156:159], v[140:143], v[72:87]\n" \
  "v_exp_f32 v120, v120\n" \
  "v_exp_f32 v121, v121\n" \
  "v_exp_f32 v122, v122\n" \
  "v_exp_f32 v123, v123\n" \
  "v_exp_f32 v124, v124\n" \
  "v_exp_f32 v125, v125\n" \
  "v_exp_f32 v126, v126\n" \
  "v_exp_f32 v127, v127\n" \
  "v_cvt_pk_bf16_f32 v136, v120, v121\n" \
  "v_cvt_pk_bf16_f32 v137, v122, v123\n" \
  "v_cvt_pk_bf16_f32 v138, v124, v125\n" \
  "v_cvt_pk_bf16_f32 v139, v126, v127\n" \
  "v_exp_f32 v128, v128\n" \
  "v_exp_f32 v129, v129\n" \
  "s_waitcnt lgkmcnt(1)\n" \
  "v_mfma_f32_32x32x16_bf16 v[72:87], v[160:163], v[136:139], v[72:87]\n" \
  "v_exp_f32 v130, v130\n" \
  "v_exp_f32 v131, v131\n" \
  "v_exp_f32 v132, v132\n" \
  "v_exp_f32 v133, v133\n" \
  "v_exp_f32 v134, v134\n" \
  "v_exp_f32 v135, v135\n" \
  "v_cvt_pk_bf16_f32 v140, v128, v129\n" \
  "v_cvt_pk_bf16_f32 v141, v130, v131\n" \
  "v_cvt_pk_bf16_f32 v142, v132, v133\n" \
  "v_cvt_pk_bf16_f32 v143, v134, v135\n" \
  "s_waitcnt lgkmcnt(0)\n" \
  "s_nop 0\n" \
  "v_mfma_f32_32x32x16_bf16 v[72:87], v[164:167], v[140:143], v[72:87]\n" \
  "s_nop 7\n" \
  "s_nop 3\n" \
  "s_waitcnt lgkmcnt(0)\n" \
  "s_mov_b32 m0, s49\n" \
  ""
#define ATT_ASM_PRE \
  "s_nop 1\n" \
  "v_mov_b32 v88, v68\n" \
  "v_mov_b32 v89, v68\n" \
  "v_mov_b32 v90, v68\n" \
  "v_mov_b32 v91, v68\n" \
  "v_mov_b32 v92, v68\n" \
  "v_mov_b32 v93, v68\n" \
  "v_mov_b32 v94, v68\n" \
  "v_mov_b32 v95, v68\n" \
  "v_mov_b32 v96, v68\n" \
  "v_mov_b32 v97, v68\n" \
  "v_mov_b32 v98, v68\n" \
  "v_mov_b32 v99, v68\n" \
  "v_mov_b32 v100, v68\n" \
  "v_mov_b32 v101, v68\n" \
  "v_mov_b32 v102, v68\n" \
  "v_mov_b32 v103, v68\n" \
  "ds_read_b128 v[144:147], v69\n" \
  "ds_read_b128 v[148:151], v69 offset:4096\n" \
  "ds_read_b128 v[152:155], v70\n" \
  "ds_read_b128 v[156:159], v70 offset:512\n" \
  "v_mov_b64 v[72:73], 0\n" \
  "v_mov_b64 v[74:75], 0\n" \
  "v_mov_b64 v[76:77], 0\n" \
  "v_mov_b64 v[78:79], 0\n" \
  "v_mov_b64 v[80:81], 0\n" \
  "v_mov_b64 v[82:83], 0\n" \
  "v_mov_b64 v[84:85], 0\n" \
  "v_mov_b64 v[86:87], 0\n" \
  "s_waitcnt lgkmcnt(3)\n" \
  "v_mfma_f32_32x32x16_bf16 v[104:119], v[144:147], v[64:67], v[88:103]\n" \
  "s_waitcnt lgkmcnt(2)\n" \
  "v_mfma_f32_32x32x16_bf16 v[120:135], v[148:151], v[64:67], v[88:103]\n" \
  "v_add_u32 v70, v70, v71\n" \
  "ds_read_b128 v[160:163], v70\n" \
  "ds_read_b128 v[164:167], v70 offset:512\n" \
  "ds_read_b128 v[144:147], v69 offset:8192\n" \
  "s_nop 5\n" \
  "v_exp_f32 v104, v104\n" \
  "v_exp_f32 v105, v105\n" \
  "v_exp_f32 v106, v106\n" \
  "v_exp_f32 v107, v107\n" \
  "v_exp_f32 v108, v108\n" \
  "v_exp_f32 v109, v109\n" \
  "v_exp_f32 v110, v110\n" \
  "v_exp_f32 v111, v111\n" \
  "v_cvt_pk_bf16_f32 v136, v104, v105\n" \
  "v_cvt_pk_bf16_f32 v137, v106, v107\n" \
  "v_cvt_pk_bf16_f32 v138, v108, v109\n" \
  "v_cvt_pk_bf16_f32 v139, v110, v111\n" \
  "v_exp_f32 v112, v112\n" \
  "v_exp_f32 v113, v113\n" \
  "s_waitcnt lgkmcnt(4)\n" \
  "v_mfma_f32_32x32x16_bf16 v[72:87], v[152:155], v[136:139], v[72:87]\n" \
  "v_exp_f32 v114, v114\n" \
  "v_exp_f32 v115, v115\n" \
  "v_exp_f32 v116, v116\n" \
  "v_exp_f32 v117, v117\n" \
  "v_exp_f32 v118, v118\n" \
  "v_exp_f32 v119, v119\n" \
  "v_cvt_pk_bf16_f32 v140, v112, v113\n" \
  "v_cvt_pk_bf16_f32 v141, v114, v115\n" \
  "v_cvt_pk_bf16_f32 v142, v116, v117\n" \
  "v_cvt_pk_bf16_f32 v143, v118, v119\n" \
  "s_waitcnt lgkmcnt(3)\n" \
  "s_nop 0\n" \
  "v_mfma_f32_32x32x16_bf16 v[72:87], v[156:159], v[140:143], v[72:87]\n" \
  "s_waitcnt lgkmcnt(0)\n" \
  "v_mfma_f32_32x32x16_bf16 v[104:119], v[144:147], v[64:67], v[88:103]\n" \
  "v_add_u32 v70, v70, v71\n" \
  "ds_read_b128 v[152:155], v70\n" \
  "ds_read_b128 v[156:159], v70 offset:512\n" \
  "ds_read_b128 v[148:151], v69 offset:12288\n" \
  "v_exp_f32 v120, v120\n" \
  "v_exp_f32 v121, v121\n" \
  "v_exp_f32 v122, v122\n" \
  "v_exp_f32 v123, v123\n" \
  "v_exp_f32 v124, v124\n" \
  "v_exp_f32 v125, v125\n" \
  "v_exp_f32 v126, v126\n" \
  "v_exp_f32 v127, v127\n" \
  "v_cvt_pk_bf16_f32 v136, v120, v121\n" \
  "v_cvt_pk_bf16_f32 v137, v122, v123\n" \
  "v_cvt_pk_bf16_f32 v138, v124, v125\n" \
  "v_cvt_pk_bf16_f32 v139, v126, v127\n" \
  "v_exp_f32 v128, v128\n" \
  "v_exp_f32 v129, v129\n" \
  "v_mfma_f32_32x32x16_bf16 v[72:87], v[160:163], v[136:139], v[72:87]\n" \
  "v_exp_f32 v130, v130\n" \
  "v_exp_f32 v131, v131\n" \
  "v_exp_f32 v132, v132\n" \
  "v_exp_f32 v133, v133\n" \
  "v_exp_f32 v134, v134\n" \
  "v_exp_f32 v135, v135\n" \
  "v_cvt_pk_bf16_f32 v140, v128, v129\n" \
  "v_cvt_pk_bf16_f32 v141, v130, v131\n" \
  "v_cvt_pk_bf16_f32 v142, v132, v133\n" \
  "v_cvt_pk_bf16_f32 v143, v134, v135\n" \
  "s_nop 0\n" \
  "s_nop 0\n" \
  "v_mfma_f32_32x32x16_bf16 v[72:87], v[164:167], v[140:143], v[72:87]\n" \
  "s_waitcnt lgkmcnt(0)\n" \
  "v_mfma_f32_32x32x16_bf16 v[120:135], v[148:151], v[64:67], v[88:103]\n" \
  "v_add_u32 v70, v70, v71\n" \
  "ds_read_b128 v[160:163], v70\n" \
  "ds_read_b128 v[164:167], v70 offset:512\n" \
  "ds_read_b128 v[144:147], v69 offset:16384\n" \
  "v_exp_f32 v104, v104\n" \
  "v_exp_f32 v105, v105\n" \
  "v_exp_f32 v106, v106\n" \
  "v_exp_f32 v107, v107\n" \
  "v_exp_f32 v108, v108\n" \
  "v_exp_f32 v109, v109\n" \
  "v_exp_f32 v110, v110\n" \
  "v_exp_f32 v111, v111\n" \
  "v_cvt_pk_bf16_f32 v136, v104, v105\n" \
  "v_cvt_pk_bf16_f32 v137, v106, v107\n" \
  "v_cvt_pk_bf16_f32 v138, v108, v109\n" \
  "v_cvt_pk_bf16_f32 v139, v110, v111\n" \
  "v_exp_f32 v112, v112\n" \
  "v_exp_f32 v113, v113\n" \
  "v_mfma_f32_32x32x16_bf16 v[72:87], v[152:155], v[136:139], v[72:87]\n" \
  "v_exp_f32 v114, v114\n" \
  "v_exp_f32 v115, v115\n" \
  "v_exp_f32 v116, v116\n" \
  "v_exp_f32 v117, v117\n" \
  "v_exp_f32 v118, v118\n" \
  "v_exp_f32 v119, v119\n" \
  "v_cvt_pk_bf16_f32 v140, v112, v113\n" \
  "v_cvt_pk_bf16_f32 v141, v114, v115\n" \
  "v_cvt_pk_bf16_f32 v142, v116, v117\n" \
  "v_cvt_pk_bf16_f32 v143, v118, v119\n" \
  "s_nop 0\n" \
  "s_nop 0\n" \
  "v_mfma_f32_32x32x16_bf16 v[72:87], v[156:159], v[140:143], v[72:87]\n" \
  "s_waitcnt lgkmcnt(0)\n" \
  "v_mfma_f32_32x32x16_bf16 v[104:119], v[144:147], v[64:67], v[88:103]\n" \
  "v_add_u32 v70, v70, v71\n" \
  "ds_read_b128 v[152:155], v70\n" \
  "ds_read_b128 v[156:159], v70 offset:512\n" \
  "ds_read_b128 v[148:151], v69 offset:20480\n" \
  "v_exp_f32 v120, v120\n" \
  "v_exp_f32 v121, v121\n" \
  "v_exp_f32 v122, v122\n" \
  "v_exp_f32 v123, v123\n" \
  "v_exp_f32 v124, v124\n" \
  "v_exp_f32 v125, v125\n" \
  "v_exp_f32 v126, v126\n" \
  "v_exp_f32 v127, v127\n" \
  "v_cvt_pk_bf16_f32 v136, v120, v121\n" \
  "v_cvt_pk_bf16_f32 v137, v122, v123\n" \
  "v_cvt_pk_bf16_f32 v138, v124, v125\n" \
  "v_cvt_pk_bf16_f32 v139, v126, v127\n" \
  "v_exp_f32 v128, v128\n" \
  "v_exp_f32 v129, v129\n" \
  "v_mfma_f32_32x32x16_bf16 v[72:87], v[160:163], v[136:139], v[72:87]\n" \
  "v_exp_f32 v130, v130\n" \
  "v_exp_f32 v131, v131\n" \
  "v_exp_f32 v132, v132\n" \
  "v_exp_f32 v133, v133\n" \
  "v_exp_f32 v134, v134\n" \
  "v_exp_f32 v135, v135\n" \
  "v_cvt_pk_bf16_f32 v140, v128, v129\n" \
  "v_cvt_pk_bf16_f32 v141, v130, v131\n" \
  "v_cvt_pk_bf16_f32 v142, v132, v133\n" \
  "v_cvt_pk_bf16_f32 v143, v134, v135\n" \
  "s_nop 0\n" \
  "s_nop 0\n" \
  "v_mfma_f32_32x32x16_bf16 v[72:87], v[164:167], v[140:143], v[72:87]\n" \
  "s_waitcnt lgkmcnt(0)\n" \
  "v_mfma_f32_32x32x16_bf16 v[120:135], v[148:151], v[64:67], v[88:103]\n" \
  "v_add_u32 v70, v70, v71\n" \
  "ds_read_b128 v[160:163], v70\n" \
  "ds_read_b128 v[164:167], v70 offset:512\n" \
  "ds_read_b128 v[144:147], v69 offset:24576\n" \
  "v_exp_f32 v104, v104\n" \
  "v_exp_f32 v105, v105\n" \
  "v_exp_f32 v106, v106\n" \
  "v_exp_f32 v107, v107\n" \
  "v_exp_f32 v108, v108\n" \
  "v_exp_f32 v109, v109\n" \
  "v_exp_f32 v110, v110\n" \
  "v_exp_f32 v111, v111\n" \
  "v_cvt_pk_bf16_f32 v136, v104, v105\n" \
  "v_cvt_pk_bf16_f32 v137, v106, v107\n" \
  "v_cvt_pk_bf16_f32 v138, v108, v109\n" \
  "v_cvt_pk_bf16_f32 v139, v110, v111\n" \
  "v_exp_f32 v112, v112\n" \
  "v_exp_f32 v113, v113\n" \
  "v_mfma_f32_32x32x16_bf16 v[72:87], v[152:155], v[136:139], v[72:87]\n" \
  "v_exp_f32 v114, v114\n" \
  "v_exp_f32 v115, v115\n" \
  "v_exp_f32 v116, v116\n" \
  "v_exp_f32 v117, v117\n" \
  "v_exp_f32 v118, v118\n" \
  "v_exp_f32 v119, v119\n" \
  "v_cvt_pk_bf16_f32 v140, v112, v113\n" \
  "v_cvt_pk_bf16_f32 v141, v114, v115\n" \
  "v_cvt_pk_bf16_f32 v142, v116, v117\n" \
  "v_cvt_pk_bf16_f32 v143, v118, v119\n" \
  "s_nop 0\n" \
  "s_nop 0\n" \
  "v_mfma_f32_32x32x16_bf16 v[72:87], v[156:159], v[140:143], v[72:87]\n" \
  "s_waitcnt lgkmcnt(0)\n" \
  "v_mfma_f32_32x32x16_bf16 v[104:119], v[144:147], v[64:67], v[88:103]\n" \
  "v_add_u32 v70, v70, v71\n" \
  "ds_read_b128 v[152:155], v70\n" \
  "ds_read_b128 v[156:159], v70 offset:512\n" \
  "ds_read_b128 v[148:151], v69 offset:28672\n" \
  "v_exp_f32 v120, v120\n" \
  "v_exp_f32 v121, v121\n" \
  "v_exp_f32 v122, v122\n" \
  "v_exp_f32 v123, v123\n" \
  "v_exp_f32 v124, v124\n" \
  "v_exp_f32 v125, v125\n" \
  "v_exp_f32 v126, v126\n" \
  "v_exp_f32 v127, v127\n" \
  "v_cvt_pk_bf16_f32 v136, v120, v121\n" \
  "v_cvt_pk_bf16_f32 v137, v122, v123\n" \
  "v_cvt_pk_bf16_f32 v138, v124, v125\n" \
  "v_cvt_pk_bf16_f32 v139, v126, v127\n" \
  "v_exp_f32 v128, v128\n" \
  "v_exp_f32 v129, v129\n" \
  "v_mfma_f32_32x32x16_bf16 v[72:87], v[160:163], v[136:139], v[72:87]\n" \
  "v_exp_f32 v130, v130\n" \
  "v_exp_f32 v131, v131\n" \
  "v_exp_f32 v132, v132\n" \
  "v_exp_f32 v133, v133\n" \
  "v_exp_f32 v134, v134\n" \
  "v_exp_f32 v135, v135\n" \
  "v_cvt_pk_bf16_f32 v140, v128, v129\n" \
  "v_cvt_pk_bf16_f32 v141, v130, v131\n" \
  "v_cvt_pk_bf16_f32 v142, v132, v133\n" \
  "v_cvt_pk_bf16_f32 v143, v134, v135\n" \
  "s_nop 0\n" \
  "s_nop 0\n" \
  "v_mfma_f32_32x32x16_bf16 v[72:87], v[164:167], v[140:143], v[72:87]\n" \
  "s_waitcnt lgkmcnt(0)\n" \
  "v_mfma_f32_32x32x16_bf16 v[120:135], v[148:151], v[64:67], v[88:103]\n" \
  "v_add_u32 v70, v70, v71\n" \
  "ds_read_b128 v[160:163], v70\n" \
  "ds_read_b128 v[164:167], v70 offset:512\n" \
  "ds_read_b128 v[144:147], v69 offset:32768\n" \
  "v_exp_f32 v104, v104\n" \
  "v_exp_f32 v105, v105\n" \
  "v_exp_f32 v106, v106\n" \
  "v_exp_f32 v107, v107\n" \
  "v_exp_f32 v108, v108\n" \
  "v_exp_f32 v109, v109\n" \
  "v_exp_f32 v110, v110\n" \
  "v_exp_f32 v111, v111\n" \
  "v_cvt_pk_bf16_f32 v136, v104, v105\n" \
  "v_cvt_pk_bf16_f32 v137, v106, v107\n" \
  "v_cvt_pk_bf16_f32 v138, v108, v109\n" \
  "v_cvt_pk_bf16_f32 v139, v110, v111\n" \
  "v_exp_f32 v112, v112\n" \
  "v_exp_f32 v113, v113\n" \
  "v_mfma_f32_32x32x16_bf16 v[72:87], v[152:155], v[136:139], v[72:87]\n" \
  "v_exp_f32 v114, v114\n" \
  "v_exp_f32 v115, v115\n" \
  "v_exp_f32 v116, v116\n" \
  "v_exp_f32 v117, v117\n" \
  "v_exp_f32 v118, v118\n" \
  "v_exp_f32 v119, v119\n" \
  "v_cvt_pk_bf16_f32 v140, v112, v113\n" \
  "v_cvt_pk_bf16_f32 v141, v114, v115\n" \
  "v_cvt_pk_bf16_f32 v142, v116, v117\n" \
  "v_cvt_pk_bf16_f32 v143, v118, v119\n" \
  "s_nop 0\n" \
  "s_nop 0\n" \
  "v_mfma_f32_32x32x16_bf16 v[72:87], v[156:159], v[140:143], v[72:87]\n" \
  "s_waitcnt lgkmcnt(0)\n" \
  "v_mfma_f32_32x32x16_bf16 v[104:119], v[144:147], v[64:67], v[88:103]\n" \
  "v_add_u32 v70, v70, v71\n" \
  "ds_read_b128 v[152:155], v70\n" \
  "ds_read_b128 v[156:159], v70 offset:512\n" \
  "ds_read_b128 v[148:151], v69 offset:36864\n" \
  "v_exp_f32 v120, v120\n" \
  "v_exp_f32 v121, v121\n" \
  "v_exp_f32 v122, v122\n" \
  "v_exp_f32 v123, v123\n" \
  "v_exp_f32 v124, v124\n" \
  "v_exp_f32 v125, v125\n" \
  "v_exp_f32 v126, v126\n" \
  "v_exp_f32 v127, v127\n" \
  "v_cvt_pk_bf16_f32 v136, v120, v121\n" \
  "v_cvt_pk_bf16_f32 v137, v122, v123\n" \
  "v_cvt_pk_bf16_f32 v138, v124, v125\n" \
  "v_cvt_pk_bf16_f32 v139, v126, v127\n" \
  "v_exp_f32 v128, v128\n" \
  "v_exp_f32 v129, v129\n" \
  "v_mfma_f32_32x32x16_bf16 v[72:87], v[160:163], v[136:139], v[72:87]\n" \
  "v_exp_f32 v130, v130\n" \
  "v_exp_f32 v131, v131\n" \
  "v_exp_f32 v132, v132\n" \
  "v_exp_f32 v133, v133\n" \
  "v_exp_f32 v134, v134\n" \
  "v_exp_f32 v135, v135\n" \
  "v_cvt_pk_bf16_f32 v140, v128, v129\n" \
  "v_cvt_pk_bf16_f32 v141, v130, v131\n" \
  "v_cvt_pk_bf16_f32 v142, v132, v133\n" \
  "v_cvt_pk_bf16_f32 v143, v134, v135\n" \
  "s_nop 0\n" \
  "s_nop 0\n" \
  "v_mfma_f32_32x32x16_bf16 v[72:87], v[164:167], v[140:143], v[72:87]\n" \
  "s_waitcnt lgkmcnt(0)\n" \
  "v_mfma_f32_32x32x16_bf16 v[120:135], v[148:151], v[64:67], v[88:103]\n" \
  "v_add_u32 v70, v70, v71\n" \
  "ds_read_b128 v[160:163], v70\n" \
  "ds_read_b128 v[164:167], v70 offset:512\n" \
  "ds_read_b128 v[144:147], v69 offset:40960\n" \
  "v_exp_f32 v104, v104\n" \
  "v_exp_f32 v105, v105\n" \
  "v_exp_f32 v106, v106\n" \
  "v_exp_f32 v107, v107\n" \
  "v_exp_f32 v108, v108\n" \
  "v_exp_f32 v109, v109\n" \
  "v_exp_f32 v110, v110\n" \
  "v_exp_f32 v111, v111\n" \
  "v_cvt_pk_bf16_f32 v136, v104, v105\n" \
  "v_cvt_pk_bf16_f32 v137, v106, v107\n" \
  "v_cvt_pk_bf16_f32 v138, v108, v109\n" \
  "v_cvt_pk_bf16_f32 v139, v110, v111\n" \
  "v_exp_f32 v112, v112\n" \
  "v_exp_f32 v113, v113\n" \
  "v_mfma_f32_32x32x16_bf16 v[72:87], v[152:155], v[136:139], v[72:87]\n" \
  "v_exp_f32 v114, v114\n" \
  "v_exp_f32 v115, v115\n" \
  "v_exp_f32 v116, v116\n" \
  "v_exp_f32 v117, v117\n" \
  "v_exp_f32 v118, v118\n" \
  "v_exp_f32 v119, v119\n" \
  "v_cvt_pk_bf16_f32 v140, v112, v113\n" \
  "v_cvt_pk_bf16_f32 v141, v114, v115\n" \
  "v_cvt_pk_bf16_f32 v142, v116, v117\n" \
  "v_cvt_pk_bf16_f32 v143, v118, v119\n" \
  "s_nop 0\n" \
  "s_nop 0\n" \
  "v_mfma_f32_32x32x16_bf16 v[72:87], v[156:159], v[140:143], v[72:87]\n" \
  "s_waitcnt lgkmcnt(0)\n" \
  "v_mfma_f32_32x32x16_bf16 v[104:119], v[144:147], v[64:67], v[88:103]\n" \
  "v_add_u32 v70, v70, v71\n" \
  "ds_read_b128 v[152:155], v70\n" \
  "ds_read_b128 v[156:159], v70 offset:512\n" \
  "ds_read_b128 v[148:151], v69 offset:45056\n" \
  "v_exp_f32 v120, v120\n" \
  "v_exp_f32 v121, v121\n" \
  "v_exp_f32 v122, v122\n" \
  "v_exp_f32 v123, v123\n" \
  "v_exp_f32 v124, v124\n" \
  "v_exp_f32 v125, v125\n" \
  "v_exp_f32 v126, v126\n" \
  "v_exp_f32 v127, v127\n" \
  "v_cvt_pk_bf16_f32 v136, v120, v121\n" \
  "v_cvt_pk_bf16_f32 v137, v122, v123\n" \
  "v_cvt_pk_bf16_f32 v138, v124, v125\n" \
  "v_cvt_pk_bf16_f32 v139, v126, v127\n" \
  "v_exp_f32 v128, v128\n" \
  "v_exp_f32 v129, v129\n" \
  "v_mfma_f32_32x32x16_bf16 v[72:87], v[160:163], v[136:139], v[72:87]\n" \
  "v_exp_f32 v130, v130\n" \
  "v_exp_f32 v131, v131\n" \
  "v_exp_f32 v132, v132\n" \
  "v_exp_f32 v133, v133\n" \
  "v_exp_f32 v134, v134\n" \
  "v_exp_f32 v135, v135\n" \
  "v_cvt_pk_bf16_f32 v140, v128, v129\n" \
  "v_cvt_pk_bf16_f32 v141, v130, v131\n" \
  "v_cvt_pk_bf16_f32 v142, v132, v133\n" \
  "v_cvt_pk_bf16_f32 v143, v134, v135\n" \
  "s_nop 0\n" \
  "s_nop 0\n" \
  "v_mfma_f32_32x32x16_bf16 v[72:87], v[164:167], v[140:143], v[72:87]\n" \
  "s_waitcnt lgkmcnt(0)\n" \
  "v_mfma_f32_32x32x16_bf16 v[120:135], v[148:151], v[64:67], v[88:103]\n" \
  "v_add_u32 v70, v70, v71\n" \
  "ds_read_b128 v[160:163], v70\n" \
  "ds_read_b128 v[164:167], v70 offset:512\n" \
  "ds_read_b128 v[144:147], v69 offset:49152\n" \
  "v_exp_f32 v104, v104\n" \
  "v_exp_f32 v105, v105\n" \
  "v_exp_f32 v106, v106\n" \
  "v_exp_f32 v107, v107\n" \
  "v_exp_f32 v108, v108\n" \
  "v_exp_f32 v109, v109\n" \
  "v_exp_f32 v110, v110\n" \
  "v_exp_f32 v111, v111\n" \
  "v_cvt_pk_bf16_f32 v136, v104, v105\n" \
  "v_cvt_pk_bf16_f32 v137, v106, v107\n" \
  "v_cvt_pk_bf16_f32 v138, v108, v109\n" \
  "v_cvt_pk_bf16_f32 v139, v110, v111\n" \
  "v_exp_f32 v112, v112\n" \
  "v_exp_f32 v113, v113\n" \
  "v_mfma_f32_32x32x16_bf16 v[72:87], v[152:155], v[136:139], v[72:87]\n" \
  "v_exp_f32 v114, v114\n" \
  "v_exp_f32 v115, v115\n" \
  "v_exp_f32 v116, v116\n" \
  "v_exp_f32 v117, v117\n" \
  "v_exp_f32 v118, v118\n" \
  "v_exp_f32 v119, v119\n" \
  "v_cvt_pk_bf16_f32 v140, v112, v113\n" \
  "v_cvt_pk_bf16_f32 v141, v114, v115\n" \
  "v_cvt_pk_bf16_f32 v142, v116, v117\n" \
  "v_cvt_pk_bf16_f32 v143, v118, v119\n" \
  "s_nop 0\n" \
  "s_nop 0\n" \
  "v_mfma_f32_32x32x16_bf16 v[72:87], v[156:159], v[140:143], v[72:87]\n" \
  "s_waitcnt lgkmcnt(0)\n" \
  "v_mfma_f32_32x32x16_bf16 v[104:119], v[144:147], v[64:67], v[88:103]\n" \
  "v_add_u32 v70, v70, v71\n" \
  "ds_read_b128 v[152:155], v70\n" \
  "ds_read_b128 v[156:159], v70 offset:512\n" \
  "ds_read_b128 v[148:151], v69 offset:53248\n" \
  "v_exp_f32 v120, v120\n" \
  "v_exp_f32 v121, v121\n" \
  "v_exp_f32 v122, v122\n" \
  "v_exp_f32 v123, v123\n" \
  "v_exp_f32 v124, v124\n" \
  "v_exp_f32 v125, v125\n" \
  "v_exp_f32 v126, v126\n" \
  "v_exp_f32 v127, v127\n" \
  "v_cvt_pk_bf16_f32 v136, v120, v121\n" \
  "v_cvt_pk_bf16_f32 v137, v122, v123\n" \
  "v_cvt_pk_bf16_f32 v138, v124, v125\n" \
  "v_cvt_pk_bf16_f32 v139, v126, v127\n" \
  "v_exp_f32 v128, v128\n" \
  "v_exp_f32 v129, v129\n" \
  "v_mfma_f32_32x32x16_bf16 v[72:87], v[160:163], v[136:139], v[72:87]\n" \
  "v_exp_f32 v130, v130\n" \
  "v_exp_f32 v131, v131\n" \
  "v_exp_f32 v132, v132\n" \
  "v_exp_f32 v133, v133\n" \
  "v_exp_f32 v134, v134\n" \
  "v_exp_f32 v135, v135\n" \
  "v_cvt_pk_bf16_f32 v140, v128, v129\n" \
  "v_cvt_pk_bf16_f32 v141, v130, v131\n" \
  "v_cvt_pk_bf16_f32 v142, v132, v133\n" \
  "v_cvt_pk_bf16_f32 v143, v134, v135\n" \
  "s_nop 0\n" \
  "s_nop 0\n" \
  "v_mfma_f32_32x32x16_bf16 v[72:87], v[164:167], v[140:143], v[72:87]\n" \
  "s_waitcnt lgkmcnt(0)\n" \
  "v_mfma_f32_32x32x16_bf16 v[120:135], v[148:151], v[64:67], v[88:103]\n" \
  "v_add_u32 v70, v70, v71\n" \
  "ds_read_b128 v[160:163], v70\n" \
  "ds_read_b128 v[164:167], v70 offset:512\n" \
  "v_exp_f32 v104, v104\n" \
  "v_exp_f32 v105, v105\n" \
  "v_exp_f32 v106, v106\n" \
  "v_exp_f32 v107, v107\n" \
  "v_exp_f32 v108, v108\n" \
  "v_exp_f32 v109, v109\n" \
  "v_exp_f32 v110, v110\n" \
  "v_exp_f32 v111, v111\n" \
  "v_cvt_pk_bf16_f32 v136, v104, v105\n" \
  "v_cvt_pk_bf16_f32 v137, v106, v107\n" \
  "v_cvt_pk_bf16_f32 v138, v108, v109\n" \
  "v_cvt_pk_bf16_f32 v139, v110, v111\n" \
  "v_exp_f32 v112, v112\n" \
  "v_exp_f32 v113, v113\n" \
  "v_mfma_f32_32x32x16_bf16 v[72:87], v[152:155], v[136:139], v[72:87]\n" \
  "v_exp_f32 v114, v114\n" \
  "v_exp_f32 v115, v115\n" \
  "v_exp_f32 v116, v116\n" \
  "v_exp_f32 v117, v117\n" \
  "v_exp_f32 v118, v118\n" \
  "v_exp_f32 v119, v119\n" \
  "v_cvt_pk_bf16_f32 v140, v112, v113\n" \
  "v_cvt_pk_bf16_f32 v141, v114, v115\n" \
  "v_cvt_pk_bf16_f32 v142, v116, v117\n" \
  "v_cvt_pk_bf16_f32 v143, v118, v119\n" \
  "s_nop 0\n" \
  "s_nop 0\n" \
  "v_mfma_f32_32x32x16_bf16 v[72:87], v[156:159], v[140:143], v[72:87]\n" \
  "v_exp_f32 v120, v120\n" \
  "v_exp_f32 v121, v121\n" \
  "v_exp_f32 v122, v122\n" \
  "v_exp_f32 v123, v123\n" \
  "v_exp_f32 v124, v124\n" \
  "v_exp_f32 v125, v125\n" \
  "v_exp_f32 v126, v126\n" \
  "v_exp_f32 v127, v127\n" \
  "v_cvt_pk_bf16_f32 v136, v120, v121\n" \
  "v_cvt_pk_bf16_f32 v137, v122, v123\n" \
  "v_cvt_pk_bf16_f32 v138, v124, v125\n" \
  "v_cvt_pk_bf16_f32 v139, v126, v127\n" \
  "v_exp_f32 v128, v128\n" \
  "v_exp_f32 v129, v129\n" \
  "s_waitcnt lgkmcnt(1)\n" \
  "v_mfma_f32_32x32x16_bf16 v[72:87], v[160:163], v[136:139], v[72:87]\n" \
  "v_exp_f32 v130, v130\n" \
  "v_exp_f32 v131, v131\n" \
  "v_exp_f32 v132, v132\n" \
  "v_exp_f32 v133, v133\n" \
  "v_exp_f32 v134, v134\n" \
  "v_exp_f32 v135, v135\n" \
  "v_cvt_pk_bf16_f32 v140, v128, v129\n" \
  "v_cvt_pk_bf16_f32 v141, v130, v131\n" \
  "v_cvt_pk_bf16_f32 v142, v132, v133\n" \
  "v_cvt_pk_bf16_f32 v143, v134, v135\n" \
  "s_waitcnt lgkmcnt(0)\n" \
  "s_nop 0\n" \
  "v_mfma_f32_32x32x16_bf16 v[72:87], v[164:167], v[140:143], v[72:87]\n" \
  "s_nop 7\n" \
  "s_nop 3\n" \
  "s_waitcnt lgkmcnt(0)\n" \
  ""

DI unsigned wsw(int row) { return ((row & 3) << 2) | ((row >> 2) & 3); }
DI void gdma16(const void* g, unsigned ldsbase) {
    unsigned sv;
    ldsbase = __builtin_amdgcn_readfirstlane(ldsbase);
    asm volatile("s_mov_b32 %0, m0\n\ts_mov_b32 m0, %2\n\ts_nop 0\n\tglobal_load_lds_dwordx4 %1, off\n\ts_mov_b32 m0, %0" : "=&s"(sv) : "v"(g), "s"(ldsbase) : "memory");
}
DI void stage_w(const u16* W, int ldw, int k0, int col0, int nrows, unsigned ldsb, int w, int lane) {
    const int rl = lane >> 4;
    const u16* src = W + (size_t)(k0 + 4 * w + rl) * ldw + col0 + (((lane & 15) ^ ((rl << 2) | w)) << 3);
    for (int r0 = 4 * w; r0 < nrows; r0 += 16) { gdma16(src, ldsb + r0 * 256); src += (size_t)16 * ldw; }
}
DI int woff(int row, int col) { return row * 256 + ((((col >> 3) ^ wsw(row))) << 4) + (col & 7) * 2; }
DI const LAS char* wimg(const LAS char* base, int row, int col) { return base + woff(row, col); }
DI bf16x8 wfrag(const LAS char* base, int k0, int col0, int c, int g) {
    const int col = col0 + (c & 3) * 4, r = k0 + 8 * g + (c >> 2);
    return cat8(trread(wimg(base, r, col)), trread(wimg(base, r + 4, col)));
}
#define VMW(n) asm volatile("s_waitcnt vmcnt(" #n ")" ::: "memory")
DI bf16x8 ones8() { u32x4 o = {0x3f803f80u, 0x3f803f80u, 0x3f803f80u, 0x3f803f80u}; return asbf8(o); }
DI float sel4(f32x4 v, int r) { const float a = (r & 1) ? v[1] : v[0], b = (r & 1) ? v[3] : v[2]; return (r & 2) ? b : a; }
#define Z4 ((f32x4){0, 0, 0, 0})

template <bool TRANS>
DI void t1_init_body(const Params& p, rsrc_t rws, gu32* ctl, LAS char* sm, int rb, int cb) {
    const int tid = threadIdx.x, lane = tid & 63, c = lane & 15, g = lane >> 4;
    const int w = __builtin_amdgcn_readfirstlane(tid >> 6);
    const int m0 = rb * 128, n0 = cb * 128;
    const unsigned ldsb = (unsigned)(uintptr_t)sm;
    LAS char* sA = sm;
    LAS char* sB = sm + 98304;
    const u16* asrc = p.x + (size_t)(m0 + 4 * w + (lane >> 4)) * D + (((lane & 15) ^ ((4 * w + (lane >> 4)) & 15)) << 3);
#define STAGE_A(kq) do { _Pragma("unroll") for (int i = 0; i < 8; ++i) gdma16(asrc + (size_t)i * 16 * D + (kq) * 128, ldsb + ((kq) % 3) * 32768 + (4 * w + 16 * i) * 256); } while (0)
#define STAGE_B(n) stage_w(p.wqkv, 1536, (n) * 64, n0, 64, ldsb + 98304 + ((n) & 3) * 16384, w, lane)
    STAGE_B(0); STAGE_A(0); STAGE_B(1); STAGE_A(1); STAGE_B(2);
    const int wm = (w >> 1), wn = (w & 1);
    f32x4 acc[4][4], rsum[2], rsq[2], csum[2];
#pragma unroll
    for (int i = 0; i < 4; ++i)
#pragma unroll
        for (int j = 0; j < 4; ++j) acc[i][j] = Z4;
#pragma unroll
    for (int i = 0; i < 2; ++i) { rsum[i] = Z4; rsq[i] = Z4; csum[i] = Z4; }
    const bf16x8 one = ones8();
    const int ro = wn * 32, co = wm * 32;
    int aoff[4], boff[4][2];
#pragma unroll
    for (int i = 0; i < 4; ++i) { const int row = wm * 64 + ((i * 16 + ro) & 63) + c; aoff[i] = row * 256 + ((g ^ c) << 4); }
#pragma unroll
    for (int j = 0; j < 4; ++j) { const int col = wn * 64 + ((j * 16 + co) & 63) + (c & 3) * 4, r = 8 * g + (c >> 2);
        boff[j][0] = woff(r, col); boff[j][1] = woff(r + 4, col); }
#pragma unroll
    for (int n = 0; n < 8; ++n) {
        if (n == 0 || n == 2 || n == 4 || n == 5) VMW(16); else if (n == 1 || n == 3) VMW(24); else if (n == 6) VMW(4); else VMW(0);
        asm volatile("s_barrier" ::: "memory");
        if (n + 3 < 8) STAGE_B(n + 3);
        if (n == 0) STAGE_A(2);
        if (n == 2) STAGE_A(3);
        const LAS char* bB = sB + (n & 3) * 16384;
        bf16x8 af[2][4], wf[2][4];
#pragma unroll
        for (int kk = 0; kk < 2; ++kk) {
            const int m = (n & 1) * 2 + kk;
#pragma unroll
            for (int i = 0; i < 4; ++i) af[kk][i] = asbf8(ldsr(sA + ((n >> 1) % 3) * 32768 + (aoff[i] ^ (m << 6))));
#pragma unroll
            for (int j = 0; j < 4; ++j) wf[kk][j] = cat8(trread(bB + kk * 32 * 256 + boff[j][0]), trread(bB + kk * 32 * 256 + boff[j][1]));
        }
#pragma unroll
        for (int kk = 0; kk < 2; ++kk) {
#pragma unroll
            for (int i = 0; i < 4; ++i)
#pragma unroll
                for (int j = 0; j < 4; ++j) acc[i][j] = TRANS ? MFMA16(wf[kk][j], af[kk][i], acc[i][j]) : MFMA16(af[kk][i], wf[kk][j], acc[i][j]);
#pragma unroll
            for (int i = 0; i < 2; ++i) { rsum[i] = MFMA16(af[kk][i], one, rsum[i]); rsq[i] = MFMA16(af[kk][i], af[kk][i], rsq[i]); csum[i] = MFMA16(one, wf[kk][i], csum[i]); }
        }
        asm volatile("s_waitcnt lgkmcnt(0)" ::: "memory");
    }
#undef STAGE_A
#undef STAGE_B
    asm volatile("s_barrier" ::: "memory");
    LAS float* st = (LAS float*)sB;
#pragma unroll
    for (int i = 0; i < 2; ++i) {
        const int rowb = wm * 64 + ((i * 16 + ro) & 63);
        if (c == 0) *(LAS f32x4*)(st + rowb + 4 * g) = rsum[i] * (1.0f / D);
        if (g == (c >> 2)) st[128 + rowb + c] = sel4(rsq[i], c & 3) * (1.0f / D);
        if (g == 0) st[256 + wn * 64 + ((i * 16 + co) & 63) + c] = csum[i][0];
    }
    LBAR();
    if (tid < 128) { const float mu = st[tid]; st[128 + tid] = 1.0f / sqrtf(fmaxf(st[128 + tid] - mu * mu, 0.f) + 1e-5f); }
    LBAR();
    const int which = cb >> 2;
    float kn[4] = {0.f, 0.f, 0.f, 0.f};
#pragma unroll
    for (int i = 0; i < 4; ++i)
#pragma unroll
        for (int j = 0; j < 4; ++j) {
            const int rt = wm * 64 + ((i * 16 + ro) & 63), ct = wn * 64 + ((j * 16 + co) & 63);
            const int h = ((n0 + ct) & 511) >> 4;
            if (TRANS) {
                const int rl = rt + c; const float mu = st[rl], rs = st[128 + rl] * (which == 0 ? CA : 1.0f);
                const f32x4 cs = *(const LAS f32x4*)(st + 256 + ct + 4 * g);
                const f32x4 y = (acc[i][j] - mu * cs) * rs;
                gst8((u32x2){cvtpk(y[0], y[1]), cvtpk(y[2], y[3])}, rws, (((unsigned)h * S + m0 + rl) * 16 + 4 * g) * 2, which == 0 ? OFF_QA : OFF_KA);
                if (which == 1) kn[j] = fmaxf(kn[j], xsum32(xsum16(y[0] * y[0] + y[1] * y[1] + y[2] * y[2] + y[3] * y[3])));
            } else {
                const int rl = rt + 4 * g; const f32x4 mu = *(const LAS f32x4*)(st + rl), rs = *(const LAS f32x4*)(st + 128 + rl);
                const float cs = st[256 + ct + c];
                const f32x4 y = (acc[i][j] - mu * cs) * rs;
                gst8((u32x2){cvtpk(y[0], y[1]), cvtpk(y[2], y[3])}, rws, h * 65536 + ((m0 + rt) >> 4) * 512 + ((g & 1) * 16 + c) * 16 + (g >> 1) * 8, OFF_VA);
            }
        }
    if (TRANS && which == 1) {
#pragma unroll
        for (int j = 0; j < 4; ++j) {
            float m = kn[j];
            m = fmaxf(m, __uint_as_float(__builtin_amdgcn_update_dpp(0u, __float_as_uint(m), 0x128, 0xf, 0xf, false)));
            m = fmaxf(m, __uint_as_float(__builtin_amdgcn_update_dpp(0u, __float_as_uint(m), 0x124, 0xf, 0xf, false)));
            m = fmaxf(m, __uint_as_float(__builtin_amdgcn_update_dpp(0u, __float_as_uint(m), 0x122, 0xf, 0xf, false)));
            m = fmaxf(m, __uint_as_float(__builtin_amdgcn_update_dpp(0u, __float_as_uint(m), 0x121, 0xf, 0xf, false)));
            const int h = ((n0 + wn * 64 + ((j * 16 + co) & 63)) & 511) >> 4;
            if (lane == 0) __hip_atomic_store(ctl + C_KN + h * 32 + rb * 2 + wm, __float_as_uint(m), RLX_AGENT);
        }
    }
}
DI void role_t1_init(const Params& p, rsrc_t rws, gu32* ctl, LAS char* sm, int rb, int cb) {
    const int task = rb * 12 + cb; (void)task;
    if (cb < 8) t1_init_body<true>(p, rws, ctl, sm, rb, cb); else t1_init_body<false>(p, rws, ctl, sm, rb, cb);
    publish(ctl, C_T1P + cb * 16 + rb, 1u);
}


DI void stage_x32(rsrc_t rws, unsigned soff, unsigned ldsb, int w, int lane) {
#pragma unroll
    for (int i = 0; i < 8; ++i) { const int row = w + 4 * i;
        dma16(rws, ((lane & ~15) + ((lane & 15) ^ (row & 15))) * 16, soff + row * 1024, ldsb + row * 1024); }
}
DI int xoff(int ks, int c, int g) { const int ch = ks * 4 + g; return c * 1024 + (((ch & ~15) + ((ch & 15) ^ c)) << 4); }
DI void put_stats(LAS float* st, const f32x4 (&rsum)[2], const f32x4 (&rsq)[2], int w, int c, int g) {
#pragma unroll
    for (int rt = 0; rt < 2; ++rt) {
        if (c == 0) {
#pragma unroll
            for (int r = 0; r < 4; ++r) st[(w * 32 + rt * 16 + 4 * g + r) * 2] = rsum[rt][r]; }
        if (g == (c >> 2)) st[(w * 32 + rt * 16 + c) * 2 + 1] = sel4(rsq[rt], c & 3);
    }
}
DI void get_stats(const LAS float* st, int row, float& mu, float& rs) {
    float a = 0, b = 0;
#pragma unroll
    for (int w = 0; w < 4; ++w) { const f32x2 v = *(const LAS f32x2*)(st + (w * 32 + row) * 2); a += v[0]; b += v[1]; }
    mu = a * (1.0f / D); rs = __builtin_amdgcn_rsqf(fmaxf(b * (1.0f / D) - mu * mu, 0.f) + 1e-5f);
}
DI void get_stats4(const LAS float* st, int rt, int lane, f32x4& mu, f32x4& rs) {
    float m, r; get_stats(st, rt * 16 + (lane & 15), m, r);
    const int src = (lane >> 4) * 4;
#pragma unroll
    for (int i = 0; i < 4; ++i) { mu[i] = __shfl(m, src + i); rs[i] = __shfl(r, src + i); }
}
DI void x_stats(const LAS char* sx, LAS float* st, int w, int c, int g) {
    const bf16x8 one = ones8();
    bf16x8 xs[2][4];
#pragma unroll
    for (int i = 0; i < 4; ++i) { const int o = xoff(4 * w + i, c, g); xs[0][i] = asbf8(ldsr(sx + o)); xs[1][i] = asbf8(ldsr(sx + o + 16384)); }
    f32x4 rsum[2] = {Z4, Z4}, rsq[2] = {Z4, Z4};
#pragma unroll
    for (int i = 0; i < 4; ++i)
#pragma unroll
        for (int rt = 0; rt < 2; ++rt) { rsum[rt] = MFMA16(xs[rt][i], one, rsum[rt]); rsq[rt] = MFMA16(xs[rt][i], xs[rt][i], rsq[rt]); }
    put_stats(st, rsum, rsq, w, c, g);
}
template <bool TRANS>
DI void proj32(const LAS char* sx, const bf16x8 (&wt)[2][16], f32x4 (&acc)[2][2], int c, int g) {
    bf16x8 xf[16][2];
    const int o0 = xoff(0, c, g);
#define XLD(ks) do { const int o_ = (o0 ^ (((ks) & 3) << 6)) + ((ks) >> 2) * 256; xf[ks][0] = asbf8(ldsr(sx + o_)); xf[ks][1] = asbf8(ldsr(sx + o_ + 16384)); } while (0)
    XLD(0); XLD(1); XLD(2);
#pragma unroll
    for (int ks = 0; ks < 16; ++ks) {
        if (ks + 3 < 16) XLD(ks + 3);
        SBAR();
#pragma unroll
        for (int rt = 0; rt < 2; ++rt)
#pragma unroll
            for (int t = 0; t < 2; ++t) acc[t][rt] = TRANS ? MFMA16(wt[t][ks], xf[ks][rt], acc[t][rt]) : MFMA16(xf[ks][rt], wt[t][ks], acc[t][rt]);
        SBAR();
    }
#undef XLD
}
struct Known2 { unsigned k0, k1; int layer; };
DI void known_layer(Known2& k, int layer) { if (k.layer != layer) { k.layer = layer; k.k0 = 0; k.k1 = 0; } }
DI void wait2_wg(gu32* ctl, int base, int stride, int nw, int n, Known2& k, int lane, int w, LAS unsigned* slot, int nap) {
    const int layer = n >> 6, s = n & 63; known_layer(k, layer);
    if (s & 1) wait_words_wg(ctl, base + (layer * 2 + 1) * stride, nw, (s >> 1) + 1, k.k1, lane, w, slot, nap); else wait_words_wg(ctl, base + (layer * 2) * stride, nw, (s >> 1) + 1, k.k0, lane, w, slot, nap);
}

constexpr int AL_K = 0, AL_V = 65536, AL_S = 131072  , AL_ST = AL_S + 24576  ,
              AL_C = AL_ST + 1024  , AL_CS = AL_C + 1024  , AL_R = AL_CS + 256;
static_assert(AL_R + 64 <= LDS_BYTES - 4096, "lds");
#define ATT_CLOBBERS "memory", \
          "v88","v89","v90","v91","v92","v93","v94","v95","v96","v97","v98","v99","v100","v101","v102","v103","v104","v105","v106","v107","v108","v109","v110","v111", \
          "v112","v113","v114","v115","v116","v117","v118","v119","v120","v121","v122","v123","v124","v125","v126","v127","v128","v129","v130","v131","v132","v133","v134","v135", \
          "v136","v137","v138","v139","v140","v141","v142","v143","v144","v145","v146","v147","v148","v149","v150","v151","v152","v153","v154","v155","v156","v157","v158","v159", \
          "v160","v161","v162","v163","v164","v165","v166","v167"
template <int MODE>
DI void attn_pass(rsrc_t rws, unsigned gk, unsigned gv, unsigned ldsb, u32x4 qf, float negm, int lane, int w, f32x16& o) {
    const int r = lane & 31, hi = lane >> 5;
    const unsigned wb = MODE == 2 ? w * 1024 : w * 16384;
    const unsigned kp = ldsb + AL_K + wb + r * 32 + hi * 16;
    unsigned vp = r < 16 ? ldsb + AL_V + wb + (hi * 16 + r) * 16 : ldsb + AL_C + (r == 16 ? 0 : 16);
    const unsigned vstep = r < 16 ? (MODE == 2 ? 4096 : 1024) : 0;
    if (MODE == 1) asm volatile(ATT_ASM_FIRST : "={v[72:87]}"(o), "+{v70}"(vp) : "{v[64:67]}"(qf), "{v68}"(negm), "{v69}"(kp), "{v71}"(vstep), "{v63}"(lane * 16),
                                "{s[40:43]}"(rws), "{s44}"(gk), "{s45}"(gv), "{s46}"(ldsb + AL_K + w * 16384), "{s47}"(ldsb + AL_V + w * 16384) : ATT_CLOBBERS, "s48", "s49", "scc");
    else if (MODE == 2) asm volatile(ATT_ASM_PRE : "={v[72:87]}"(o), "+{v70}"(vp) : "{v[64:67]}"(qf), "{v68}"(negm), "{v69}"(kp), "{v71}"(vstep) : ATT_CLOBBERS);
    else asm volatile(ATT_ASM : "={v[72:87]}"(o), "+{v70}"(vp) : "{v[64:67]}"(qf), "{v68}"(negm), "{v69}"(kp), "{v71}"(vstep) : ATT_CLOBBERS);
}
DI float attn_truemax(const LAS char* sK, bf16x8 qf, int lane) {
    const int r = lane & 31, hi = lane >> 5;
    f32x16 z;
#pragma unroll
    for (int i = 0; i < 16; ++i) z[i] = 0.f;
    float m = -INFINITY;
    const LAS char* kp = sK + r * 32 + hi * 16;
    for (int T = 0; T < 64; ++T) {
        const f32x16 s = MFMA32(asbf8(ldsr(kp + T * 1024)), qf, z);
#pragma unroll
        for (int i = 0; i < 16; ++i) m = fmaxf(m, s[i]);
    }
    return xmax32(m);
}
DI float qnorm(u32x4 qu) {
    float qq = 0;
#pragma unroll
    for (int j = 0; j < 4; ++j) qq += bflo(qu[j]) * bflo(qu[j]) + bfhi(qu[j]) * bfhi(qu[j]);
    return __builtin_amdgcn_sqrtf(xsum32(qq));
}
DI void role_att(const Params& p, rsrc_t rws, gu32* ctl, LAS char* sm, int h, int wi) {
    const int tid = threadIdx.x, lane = tid & 63, r = lane & 31, hi = lane >> 5, c = lane & 15, g = lane >> 4;
    const int w = __builtin_amdgcn_readfirstlane(tid >> 6);
    const unsigned ldsb = (unsigned)(uintptr_t)sm;
    LAS float* red = (LAS float*)(sm + AL_R);
    if (tid < 8) { const unsigned v = (tid & 4) ? 0u : 0x3f803f80u; ((LAS unsigned*)(sm + AL_C))[tid] = v; ((LAS unsigned*)(sm + AL_C + 512))[tid] = v; }
    __syncthreads();
    for (int layer = 0; layer < NL; ++layer) {
        float kmax;
        const unsigned qoff = (r * 16 + hi * 8) * 2, qbase = OFF_QA + layer * 2 * MB + h * S * 32;
        u32x4 qn;
        f32x16 opre; float mpre = 0.f;
        if (layer == 0) {
            unsigned k0 = 0, k1 = 0, k2 = 0;
            if (w == 0) { wait_words(ctl, C_T1P + (h >> 3) * 16, 16, 1, k0, lane, 1); wait_words(ctl, C_T1P + (4 + (h >> 3)) * 16, 16, 1, k1, lane, 1); wait_words(ctl, C_T1P + (8 + (h >> 3)) * 16, 16, 1, k2, lane, 1); }
            __syncthreads();
            qn = gld16(rws, qoff, qbase + wi * 1024);
            {
                unsigned v = __hip_atomic_load(ctl + C_KN + h * 32 + (lane & 31), RLX_AGENT);
                v = max(v, (unsigned)__builtin_amdgcn_update_dpp(0u, v, 0x128, 0xf, 0xf, false)); v = max(v, (unsigned)__builtin_amdgcn_update_dpp(0u, v, 0x124, 0xf, 0xf, false));
                v = max(v, (unsigned)__builtin_amdgcn_update_dpp(0u, v, 0x122, 0xf, 0xf, false)); v = max(v, (unsigned)__builtin_amdgcn_update_dpp(0u, v, 0x121, 0xf, 0xf, false));
                auto rr = __builtin_amdgcn_permlane16_swap(v, v, false, false); v = max(rr[0], rr[1]);
                kmax = __builtin_amdgcn_sqrtf(__uint_as_float(v)) * 1.01f;
            }
            asm volatile("" : "+v"(qn));
        } else {
            bf16x8 wq[4][3];
            {
                __syncthreads();
                LAS char* stg = sm + AL_K + w * 16384;
#pragma unroll 1
                for (int i = 0; i < 16; ++i) {
                    const int row = i * 8 + (lane >> 3), pc = lane & 7, m = min(pc >> 1, 2);
                    gdma16(p.wqkv + (size_t)(128 * w + row) * 1536 + m * 512 + h * 16 + (pc & 1) * 8, ldsb + AL_K + w * 16384 + i * 1024);
                }
                VMW(0);
#pragma unroll
                for (int ks = 0; ks < 4; ++ks)
#pragma unroll
                    for (int m = 0; m < 3; ++m) { const LAS char* q = stg + (ks * 32 + 8 * g + (c >> 2)) * 128 + m * 32 + (c & 3) * 8; wq[ks][m] = cat8(trread(q), trread(q + 512)); }
                const bf16x8 one = ones8();
                LAS float* part = (LAS float*)(sm + AL_S);
#pragma unroll
                for (int m = 0; m < 3; ++m) { f32x4 a = Z4;
#pragma unroll
                    for (int ks = 0; ks < 4; ++ks) a = MFMA16(one, wq[ks][m], a);
                    if (g == 0) part[w * 48 + m * 16 + c] = a[0]; }
                __syncthreads();
                if (tid < 48) ((LAS float*)(sm + AL_CS))[tid] = part[tid] + part[48 + tid] + part[96 + tid] + part[144 + tid];
                __syncthreads();
            }
            unsigned kr = 0, kr2 = 0;
            float kn2 = 0.f;
            bf16x8 an[2][4];
            bool pre = false, rdy1 = false;
            for (int s = 0; s < NSEG; ++s) {

                bf16x8 af[2][4];
                if (!pre) {
                    if (s >= NSEG - 3) { unsigned kx = 0; wait_words(ctl, C_PR + (red_by_g(s) ? 16 : 0), 16, s + 1, kx, lane, 0); }
                    else if (red_by_g(s)) wait_words_wg(ctl, C_PR + 16, 16, s + 1, kr2, lane, w, (LAS unsigned*)(red + 12), 0);
                    else wait_words_wg(ctl, C_PR, 16, s + 1, kr, lane, w, (LAS unsigned*)(red + 12), 2);
#pragma unroll
                    for (int ks = 0; ks < 4; ++ks)
#pragma unroll
                        for (int rt = 0; rt < 2; ++rt) an[rt][ks] = asbf8(gld16(rws, lane * 16, OFF_XB16 + ((s * 4 + w) * 8 + rt * 4 + ks) * 1024));
                }
#pragma unroll
                for (int ks = 0; ks < 4; ++ks)
#pragma unroll
                    for (int rt = 0; rt < 2; ++rt) { af[rt][ks] = an[rt][ks]; asm volatile("" : "+v"(af[rt][ks])); }

                pre = rdy1;
                if (pre) {
#pragma unroll
                    for (int ks = 0; ks < 4; ++ks)
#pragma unroll
                        for (int rt = 0; rt < 2; ++rt) an[rt][ks] = asbf8(gld16(rws, lane * 16, OFF_XB16 + (((s + 1) * 4 + w) * 8 + rt * 4 + ks) * 1024));
                }
                unsigned look = ~0u;
                if (w == 0 && s + 2 < NSEG && lane < 16) look = __hip_atomic_load(ctl + C_PR + (red_by_g(s + 2) ? 16 : 0) + lane, RLX_AGENT);
                const bf16x8 one = ones8();
                f32x4 rsum[2] = {Z4, Z4}, rsq[2] = {Z4, Z4}, a[3][2] = {{Z4, Z4}, {Z4, Z4}, {Z4, Z4}};
#pragma unroll
                for (int ks = 0; ks < 4; ++ks)
#pragma unroll
                    for (int rt = 0; rt < 2; ++rt) {
                        a[1][rt] = MFMA16(wq[ks][1], af[rt][ks], a[1][rt]);
                        a[2][rt] = MFMA16(af[rt][ks], wq[ks][2], a[2][rt]);
                        a[0][rt] = MFMA16(wq[ks][0], af[rt][ks], a[0][rt]);
                        rsum[rt] = MFMA16(af[rt][ks], one, rsum[rt]); rsq[rt] = MFMA16(af[rt][ks], af[rt][ks], rsq[rt]);
                    }

                put_stats((LAS float*)(sm + AL_ST), rsum, rsq, w, c, g);
#pragma unroll
                for (int m = 0; m < 3; ++m)
#pragma unroll
                    for (int rt = 0; rt < 2; ++rt) *(LAS f32x4*)(sm + AL_S + ((w * 6 + m * 2 + rt) * 64 + lane) * 16) = a[m][rt];
                if (w == 0) { const bool rdy = s + 2 < NSEG && __all(look >= (unsigned)(s + 3)); if (lane == 0) red[13] = rdy ? 1.f : 0.f; }

                LBAR();
                rdy1 = red[13] != 0.f;

                if (w < 2) {
                    const int rt = w;
                    f32x4 y = Z4, z = Z4;
#pragma unroll
                    for (int ww = 0; ww < 4; ++ww) { y += *(const LAS f32x4*)(sm + AL_S + ((ww * 6 + 2 + rt) * 64 + lane) * 16); z += *(const LAS f32x4*)(sm + AL_S + ((ww * 6 + rt) * 64 + lane) * 16); }
                    float mu, rs; get_stats((const LAS float*)(sm + AL_ST), rt * 16 + c, mu, rs);
                    y = (y - mu * *(const LAS f32x4*)((const LAS float*)(sm + AL_CS) + 16 + 4 * g)) * rs;
                    *(LAS u32x2*)(sm + AL_K + (s * 32 + rt * 16 + c) * 32 + g * 8) = (u32x2){cvtpk(y[0], y[1]), cvtpk(y[2], y[3])};
                    kn2 = fmaxf(kn2, xsum32(xsum16(y[0] * y[0] + y[1] * y[1] + y[2] * y[2] + y[3] * y[3])));
                    if ((s & 1) == wi) {
                        z = (z - mu * *(const LAS f32x4*)((const LAS float*)(sm + AL_CS) + 4 * g)) * (rs * CA);
                        *(u32x2*)(p.ws + OFF_QA + 2 * MB + (((unsigned)h * S + s * 32 + rt * 16 + c) * 16 + 4 * g) * 2) = (u32x2){cvtpk(z[0], z[1]), cvtpk(z[2], z[3])};
                    }
                } else {
                    const int rt = w & 1;
                    f32x4 y = Z4;
#pragma unroll
                    for (int ww = 0; ww < 4; ++ww) y += *(const LAS f32x4*)(sm + AL_S + ((ww * 6 + 4 + rt) * 64 + lane) * 16);
                    const float cc = ((const LAS float*)(sm + AL_CS))[32 + c];
                    f32x4 mu, rs; get_stats4((const LAS float*)(sm + AL_ST), rt, lane, mu, rs);
                    y = (y - mu * cc) * rs;
                    *(LAS u32x2*)(sm + AL_V + (s * 2 + rt) * 512 + ((g & 1) * 16 + c) * 16 + (g >> 1) * 8) = (u32x2){cvtpk(y[0], y[1]), cvtpk(y[2], y[3])};
                }

                LBAR();
                if (s == 55) {
                    float k2 = kn2;
                    k2 = fmaxf(k2, __uint_as_float(__builtin_amdgcn_update_dpp(0u, __float_as_uint(k2), 0x128, 0xf, 0xf, false)));
                    k2 = fmaxf(k2, __uint_as_float(__builtin_amdgcn_update_dpp(0u, __float_as_uint(k2), 0x124, 0xf, 0xf, false)));
                    k2 = fmaxf(k2, __uint_as_float(__builtin_amdgcn_update_dpp(0u, __float_as_uint(k2), 0x122, 0xf, 0xf, false)));
                    k2 = fmaxf(k2, __uint_as_float(__builtin_amdgcn_update_dpp(0u, __float_as_uint(k2), 0x121, 0xf, 0xf, false)));
                    if (lane == 0) red[w] = k2;
                    VMW(0);
                    __syncthreads();
                    qn = *(const u32x4*)(p.ws + qbase + wi * 1024 + qoff);
                    mpre = qnorm(qn) * (__builtin_amdgcn_sqrtf(fmaxf(red[0], red[1])) * 1.01f);
                    attn_pass<2>(rws, 0, 0, ldsb, qn, -mpre, lane, w, opre);
                    __syncthreads();
                }
            }
            {
                float k2 = kn2;
                k2 = fmaxf(k2, __uint_as_float(__builtin_amdgcn_update_dpp(0u, __float_as_uint(k2), 0x128, 0xf, 0xf, false)));
                k2 = fmaxf(k2, __uint_as_float(__builtin_amdgcn_update_dpp(0u, __float_as_uint(k2), 0x124, 0xf, 0xf, false)));
                k2 = fmaxf(k2, __uint_as_float(__builtin_amdgcn_update_dpp(0u, __float_as_uint(k2), 0x122, 0xf, 0xf, false)));
                k2 = fmaxf(k2, __uint_as_float(__builtin_amdgcn_update_dpp(0u, __float_as_uint(k2), 0x121, 0xf, 0xf, false)));
                if (lane == 0) red[8 + w] = k2;
                __syncthreads();
                kmax = __builtin_amdgcn_sqrtf(fmaxf(red[8], red[9])) * 1.01f;
            }
            VMW(0);
            __syncthreads();
        }
        int s = wi;
        u32x4 qu = qn, qprev = qn;
        float m = layer == 0 ? qnorm(qu) * kmax : mpre;
        if (s + 2 < NSEG) qn = layer == 0 ? gld16(rws, qoff, qbase + (s + 2) * 1024) : *(const u32x4*)(p.ws + qbase + (s + 2) * 1024 + qoff);
        int pend = -1;
        auto combine = [&](int st, int buf, bool force) -> bool {
            const int q = (lane >> 5) * 16 + c, dh = (lane >> 4) & 1;
            const LAS float* rd = (const LAS float*)(sm + AL_S + buf * 10240) + q * 20;
            f32x4 a = Z4, b = Z4; float l = 0;
#pragma unroll
            for (int ww = 0; ww < 4; ++ww) { a += *(const LAS f32x4*)(rd + ww * 640 + 8 * dh); b += *(const LAS f32x4*)(rd + ww * 640 + 8 * dh + 4); l += rd[ww * 640 + 16]; }
            const bool ok = force || (l >= 8.673617379884035e-19f && l <= 1.152921504606847e18f);
            const float il = __builtin_amdgcn_rcpf(l);
            gst16((u32x4){cvtpk(a[0] * il, a[1] * il), cvtpk(a[2] * il, a[3] * il), cvtpk(b[0] * il, b[1] * il), cvtpk(b[2] * il, b[3] * il)}, rws,
                  (lane >> 5) * 4096 + (c + 16 * (2 * (h & 1) + dh)) * 16, OFF_OA + layer * 2 * MB + ((st * 4 + (h >> 3)) * 8 + ((h & 7) >> 1)) * 1024);
            return !__any(!ok);
        };
        auto partials = [&](const f32x16& o, int buf) {
            LAS float* sc = (LAS float*)(sm + AL_S + buf * 10240) + (w * 32 + r) * 20;
            *(LAS f32x4*)(sc + 4 * hi) = (f32x4){o[0], o[1], o[2], o[3]}; *(LAS f32x4*)(sc + 8 + 4 * hi) = (f32x4){o[4], o[5], o[6], o[7]};
            if (hi == 0) sc[16] = o[8];
        };
        auto redo = [&](int st, u32x4 q, int buf) {
            const float mt = attn_truemax(sm + AL_K, asbf8(q), lane);
            f32x16 o; attn_pass<0>(rws, 0, 0, ldsb, q, -mt, lane, w, o);
            partials(o, buf); LBAR();
            if (w == 0) (void)combine(st, buf, true);
            VMW(0); LBAR();
        };
        for (int n = 0; s < NSEG; s += 2, ++n) {
            const int buf = n & 1;
            f32x16 o;

            if (n == 0 && layer == 0) attn_pass<1>(rws, OFF_KA + h * 65536 + w * 16384, OFF_VA + h * 65536 + w * 16384, ldsb, qu, -m, lane, w, o);
            else if (n == 0) {
                o = opre;
                f32x16 cm;
#pragma unroll
                for (int i = 0; i < 16; ++i) cm[i] = -m;
#pragma unroll
                for (int k2 = 0; k2 < 2; ++k2) {
                    const int T = 56 + 4 * k2 + w;
                    const f32x16 sc = MFMA32(asbf8(ldsr(sm + AL_K + T * 1024 + r * 32 + hi * 16)), asbf8(qu), cm);
                    const LAS char* vp = r < 16 ? sm + AL_V + T * 1024 + (hi * 16 + r) * 16 : sm + AL_C + (r == 16 ? 0 : 16);
                    u32x4 p0, p1;
#pragma unroll
                    for (int i = 0; i < 4; ++i) { p0[i] = cvtpk(__builtin_amdgcn_exp2f(sc[2 * i]), __builtin_amdgcn_exp2f(sc[2 * i + 1]));
                                                  p1[i] = cvtpk(__builtin_amdgcn_exp2f(sc[8 + 2 * i]), __builtin_amdgcn_exp2f(sc[8 + 2 * i + 1])); }
                    o = MFMA32(asbf8(ldsr(vp)), asbf8(p0), o); o = MFMA32(asbf8(ldsr(vp + 512)), asbf8(p1), o);
                }
            } else attn_pass<0>(rws, 0, 0, ldsb, qu, -m, lane, w, o);

            partials(o, buf);
            VMW(0);
            asm volatile("" : "+v"(qn));
            LBAR();

            if (pend >= 0) {
                if (red[4 + (buf ^ 1)] != 0.f) redo(pend, qprev, buf ^ 1);
                if (tid == 0) __hip_atomic_store(ctl + C_PA + (layer * 2 + wi) * 32 + h, (unsigned)(pend >> 1) + 1, RLX_AGENT);
                pend = -1;
            }
            const bool urgent = n < 2 || s + 2 >= NSEG;
            if (w == 0) { const bool ok = combine(s, buf, false); if (lane == 0) red[4 + buf] = ok ? 0.f : 1.f; }

            if (urgent) {
                VMW(0); LBAR();
                if (red[4 + buf] != 0.f) redo(s, qu, buf);
                if (tid == 0) __hip_atomic_store(ctl + C_PA + (layer * 2 + wi) * 32 + h, (unsigned)(s >> 1) + 1, RLX_AGENT);
            } else pend = s;

            qprev = qu; qu = qn; m = qnorm(qu) * kmax;
            if (s + 4 < NSEG) qn = layer == 0 ? gld16(rws, qoff, qbase + (s + 4) * 1024) : *(const u32x4*)(p.ws + qbase + (s + 4) * 1024 + qoff);
        }
        __syncthreads();
    }
}

DI void red_task(const Params& p, rsrc_t rws, gu32* ctl, LAS char* sm, int cb, int nn, Known2& kq, int word, int trole) {
    const int tid = threadIdx.x, lane = tid & 63;
    const int w = __builtin_amdgcn_readfirstlane(tid >> 6);
    const int layer = nn >> 6, s = nn & 63; (void)trole;
    const int row = s * 32 + ((tid >> 2) & 31), ch = tid & 3, col = cb * 32 + ch * 8;
    wait2_wg(ctl, C_PQ, 32, 32, nn, kq, lane, w, (LAS unsigned*)(sm + 40960), s >= NSEG - 2 ? 0 : 1);
    u32x4 v[8][2];
#pragma unroll
    for (int i = 0; i < 8; ++i)
#pragma unroll
        for (int rt = 0; rt < 2; ++rt) v[i][rt] = gld16(rws, lane * 16, OFF_PB + ((s * 16 + cb) * 32 + w * 8 + i) * 2048 + rt * 1024);
    f32x4 ra = Z4, rb = Z4;
    if (w < 2) { ra = __builtin_bit_cast(f32x4, gld16(rws, (row * D + col) * 4, OFF_XA32 + layer * 4 * MB)); rb = __builtin_bit_cast(f32x4, gld16(rws, (row * D + col + 4) * 4, OFF_XA32 + layer * 4 * MB)); }
    f32x4 a[2] = {Z4, Z4}, b[2] = {Z4, Z4};
#pragma unroll
    for (int i = 0; i < 8; ++i)
#pragma unroll
        for (int rt = 0; rt < 2; ++rt) { const u32x4 u = v[i][rt];
            a[rt] += (f32x4){bflo(u[0]), bfhi(u[0]), bflo(u[1]), bfhi(u[1])}; b[rt] += (f32x4){bflo(u[2]), bfhi(u[2]), bflo(u[3]), bfhi(u[3])}; }
#pragma unroll
    for (int rt = 0; rt < 2; ++rt) { *(LAS f32x4*)(sm + ((w * 2 + rt) * 2) * 1024 + lane * 16) = a[rt]; *(LAS f32x4*)(sm + ((w * 2 + rt) * 2 + 1) * 1024 + lane * 16) = b[rt]; }
    LBAR();
    if (w < 2) {
        const int rl = (tid >> 2) & 31, rt = rl >> 4;
        const LAS char* q = sm + (rt * 2 + (ch >> 1)) * 1024 + ((rl & 15) + 32 * (ch & 1)) * 16;
#pragma unroll
        for (int ww = 0; ww < 4; ++ww) { ra += *(const LAS f32x4*)(q + ww * 4096); rb += *(const LAS f32x4*)(q + ww * 4096 + 256); }
        const u32x4 hv = {cvtpk(ra[0], ra[1]), cvtpk(ra[2], ra[3]), cvtpk(rb[0], rb[1]), cvtpk(rb[2], rb[3])};
        if (layer == 0) {
            gst16(hv, rws, rt * 4096 + ((rl & 15) + 16 * ch) * 16, OFF_XB16 + ((s * 4 + (cb >> 2)) * 8 + (cb & 3)) * 1024);
            gst16(__builtin_bit_cast(u32x4, ra), rws, (row * D + col) * 4, OFF_XB32); gst16(__builtin_bit_cast(u32x4, rb), rws, (row * D + col + 4) * 4, OFF_XB32);
        } else *(u32x4*)(p.out + (size_t)row * D + col) = hv;
    }
    if (layer == 0) publish(ctl, word, s + 1); else LBAR();
}

DI unsigned probe_parity(rsrc_t rws, unsigned base) {
    unsigned lat0 = ~0u, lat1 = ~0u;
    for (int rep = 0; rep < 20; ++rep) {
        const unsigned off = base + (rep & 1) * 8192 + (rep >> 1) * 256;
        unsigned long long t0, t1; unsigned v;
        __builtin_amdgcn_raw_buffer_store_b32(rep, rws, 0, off, 16);
        asm volatile("s_waitcnt vmcnt(0)\n\ts_memtime %0\n\ts_waitcnt lgkmcnt(0)" : "=s"(t0) :: "memory");
        v = __builtin_amdgcn_raw_buffer_load_b32(rws, 0, off, 16);
        asm volatile("s_waitcnt vmcnt(0)\n\ts_memtime %0\n\ts_waitcnt lgkmcnt(0)" : "=s"(t1) : "v"(v) : "memory");
        const unsigned d = (unsigned)(t1 - t0);
        if (rep & 1) lat1 = min(lat1, d); else lat0 = min(lat0, d);
    }
    return lat1 < lat0 ? 1u : 0u;
}
DI void role_g(const Params& p, rsrc_t rws, gu32* ctl, LAS char* sm, int grp, int cb) {
    const int tid = threadIdx.x, lane = tid & 63, c = lane & 15, g = lane >> 4;
    const int w = __builtin_amdgcn_readfirstlane(tid >> 6);
    const unsigned ldsb = (unsigned)(uintptr_t)sm;
    bf16x8 wo[4][4];
    {
        const int rl = lane >> 4;
        for (int i = 0; i < 32; ++i) { const int row = 4 * i + rl; gdma16(p.wo + (size_t)(128 * w + row) * D + (cb >> 1) * 128 + (((lane & 15) ^ wsw(row)) << 3), ldsb + w * 32768 + i * 1024); }
        VMW(0);
#pragma unroll
        for (int ks = 0; ks < 4; ++ks)
#pragma unroll
            for (int t = 0; t < 4; ++t) wo[ks][t] = wfrag(sm + w * 32768, ks * 32, (cb & 1) * 64 + t * 16, c, g);
        __syncthreads();
    }
    if (grp == 0 && cb == 0 && w < 2) { const unsigned par = probe_parity(rws, (w == 0 ? OFF_PM : OFF_PM1) + MB); if (lane == 0) __hip_atomic_store(ctl + C_PAR + w * 16, 1u + par, RLX_AGENT); }
    LAS unsigned* slot = (LAS unsigned*)(sm + 40960);
    for (int layer = 0; layer < NL; ++layer) {
        unsigned ka = 0;
        for (int s = grp; s < NSEG; s += 2) {
            const int row = s * 32 + (tid >> 3), ch = tid & 7, col = cb * 64 + ch * 8;
            u32x4 r16; f32x4 ra, rb;
            if (layer == 0) r16 = *(const u32x4*)(p.x + (size_t)row * D + col);
            wait_words_wg(ctl, C_PA + (layer * 2 + grp) * 32, 32, (s >> 1) + 1, ka, lane, w, slot, s < 2 ? 0 : 1);
            bf16x8 af[2][4];
#pragma unroll
            for (int ks = 0; ks < 4; ++ks)
#pragma unroll
                for (int rt = 0; rt < 2; ++rt) af[rt][ks] = asbf8(gld16(rws, lane * 16, OFF_OA + layer * 2 * MB + ((s * 4 + w) * 8 + rt * 4 + ks) * 1024));
            if (layer == 1) {
                ra = __builtin_bit_cast(f32x4, gld16(rws, (row * D + col) * 4, OFF_XB32)); rb = __builtin_bit_cast(f32x4, gld16(rws, (row * D + col + 4) * 4, OFF_XB32));
            } else { ra = (f32x4){bflo(r16[0]), bfhi(r16[0]), bflo(r16[1]), bfhi(r16[1])}; rb = (f32x4){bflo(r16[2]), bfhi(r16[2]), bflo(r16[3]), bfhi(r16[3])}; }
            f32x4 a[2][4] = {{Z4, Z4, Z4, Z4}, {Z4, Z4, Z4, Z4}};
#pragma unroll
            for (int ks = 0; ks < 4; ++ks)
#pragma unroll
                for (int rt = 0; rt < 2; ++rt)
#pragma unroll
                    for (int t = 0; t < 4; ++t) a[rt][t] = MFMA16(wo[ks][t], af[rt][ks], a[rt][t]);
#pragma unroll
            for (int rt = 0; rt < 2; ++rt)
#pragma unroll
                for (int t = 0; t < 4; ++t) *(LAS f32x4*)(sm + ((w * 8 + rt * 4 + t) * 64 + lane) * 16) = a[rt][t];

            LBAR();

            {
                const int rl = tid >> 3, rt = rl >> 4, t = ch >> 1, g0 = 2 * (ch & 1);
                const LAS char* q = sm + ((rt * 4 + t) * 64 + (rl & 15) + 16 * g0) * 16;
#pragma unroll
                for (int ww = 0; ww < 4; ++ww) { ra += *(const LAS f32x4*)(q + ww * 8192); rb += *(const LAS f32x4*)(q + ww * 8192 + 256); }
            }
            gst16((u32x4){cvtpk(ra[0], ra[1]), cvtpk(ra[2], ra[3]), cvtpk(rb[0], rb[1]), cvtpk(rb[2], rb[3])}, rws, (row * D + col) * 2, OFF_XA16 + layer * 2 * MB);
            gst16(__builtin_bit_cast(u32x4, ra), rws, (row * D + col) * 4, OFF_XA32 + layer * 4 * MB);
            gst16(__builtin_bit_cast(u32x4, rb), rws, (row * D + col + 4) * 4, OFF_XA32 + layer * 4 * MB);

            publish(ctl, C_PG + (layer * 2 + grp) * 16 + cb, (s >> 1) + 1);
        }
        Known2 kq = {0, 0, 0};
        for (int s = RED_SPLIT; s < NSEG; ++s) if (red_by_g(s)) red_task(p, rws, ctl, sm, grp * 8 + cb, layer * 64 + s, kq, C_PR + 16 + grp * 8 + cb, 0);
    }
}

template <int MAT>
DI void role_kv(const Params& p, rsrc_t rws, gu32* ctl, LAS char* sm, int h) {
    const int tid = threadIdx.x, lane = tid & 63, c = lane & 15, g = lane >> 4;
    const int w = __builtin_amdgcn_readfirstlane(tid >> 6);
    const unsigned ldsb = (unsigned)(uintptr_t)sm;
    constexpr int KL_ST = 65536;
    LAS unsigned* slot = (LAS unsigned*)(sm + KL_ST + 2048);
    bf16x8 wt[2][16];
    const bf16x8 one = ones8();
    f32x4 cs[2];
    __syncthreads();
    stage_w(MAT == 0 ? p.Wk : p.Wv, 4096, 0, h * 128, 512, ldsb, w, lane); VMW(0); __syncthreads();
#pragma unroll
    for (int t = 0; t < 2; ++t) { cs[t] = Z4;
#pragma unroll
        for (int ks = 0; ks < 16; ++ks) { wt[t][ks] = wfrag(sm, ks * 32, w * 32 + t * 16, c, g); cs[t] = MAT == 0 ? MFMA16(wt[t][ks], one, cs[t]) : MFMA16(one, wt[t][ks], cs[t]); } }
    __syncthreads();
    Known2 kg = {0, 0, 0};
    bool pre = false;
    for (int n = 0; n < NL * NSEG; ++n) {
        const int xb = (n & 1) * 32768;
        if (!pre) { wait2_wg(ctl, C_PG, 16, 8, n, kg, lane, w, slot, (n & 63) < 2 ? 0 : 1); stage_x32(rws, OFF_XA16 + n * 32768, ldsb + xb, w, lane); }
        VMW(0); __syncthreads();

        const int n1 = n + 1;
        unsigned look = ~0u;
        if (w == 0 && n1 < NL * NSEG && lane < 8) look = __hip_atomic_load(ctl + C_PG + ((n1 >> 6) * 2 + (n1 & 1)) * 16 + lane, RLX_AGENT);
        f32x4 acc[2][2] = {{Z4, Z4}, {Z4, Z4}};

        x_stats(sm + xb, (LAS float*)(sm + KL_ST), w, c, g);

        proj32<MAT == 0>(sm + xb, wt, acc, c, g);

        if (w == 0) { const bool rdy = n1 < NL * NSEG && (n & 63) != 0 && __all(look >= (unsigned)((n1 & 63) >> 1) + 1); if (lane == 0) slot[1] = rdy; }
        LBAR();
        pre = __builtin_amdgcn_readfirstlane(slot[1]) != 0;
        const unsigned ob = n * 262144 + h * 8192;
        if (MAT == 0) {
#pragma unroll
            for (int rt = 0; rt < 2; ++rt) {
                float mu, rs; get_stats((const LAS float*)(sm + KL_ST), rt * 16 + c, mu, rs);
                if (g == 0) gst8((u32x2){__float_as_uint(mu), __float_as_uint(rs)}, rws, (rt * 16 + c) * 8, OFF_ST + (n * 32 + h) * 256);
                rs *= CB;
                const f32x4 y0 = (acc[0][rt] - mu * cs[0]) * rs, y1 = (acc[1][rt] - mu * cs[1]) * rs;
                gst16((u32x4){cvtpk(y0[0], y0[1]), cvtpk(y0[2], y0[3]), cvtpk(y1[0], y1[1]), cvtpk(y1[2], y1[3])}, rws, lane * 16, OFF_KX + ob + (rt * 4 + w) * 1024);
            }
        } else {
            f32x4 mu4[2], rs4[2];
#pragma unroll
            for (int rt = 0; rt < 2; ++rt) get_stats4((const LAS float*)(sm + KL_ST), rt, lane, mu4[rt], rs4[rt]);
#pragma unroll
            for (int t = 0; t < 2; ++t) {
                float y[2][4];
#pragma unroll
                for (int rt = 0; rt < 2; ++rt)
#pragma unroll
                    for (int r = 0; r < 4; ++r) y[rt][r] = (acc[t][rt][r] - mu4[rt][r] * cs[t][0]) * rs4[rt][r];
                gst16((u32x4){cvtpk(y[0][0], y[0][1]), cvtpk(y[0][2], y[0][3]), cvtpk(y[1][0], y[1][1]), cvtpk(y[1][2], y[1][3])}, rws, lane * 16, OFF_VX + ob + (w * 2 + t) * 1024);
            }
        }

        if (pre) stage_x32(rws, OFF_XA16 + n1 * 32768, ldsb + (xb ^ 32768), w, lane);
        if (pre) VMW(8); else VMW(0);
        __syncthreads();
        if (tid == 0) __hip_atomic_store(ctl + C_FKV + ((n >> 6) * 32 + h) * 16 + MAT, (unsigned)(n & 63) + 1, RLX_AGENT);
    }
}

DI void role_qo(const Params& p, rsrc_t rws, gu32* ctl, LAS char* sm, int h, int par) {
    const int tid = threadIdx.x, lane = tid & 63, c = lane & 15, g = lane >> 4;
    const int w = __builtin_amdgcn_readfirstlane(tid >> 6);
    const unsigned ldsb = (unsigned)(uintptr_t)sm;
    constexpr int QL_X = 0  , QL_ST = 65536, QL_Q = QL_ST + 1024  , QL_K = QL_Q + 8192  , QL_O = QL_K + 8192  ;
    LAS unsigned* slot = (LAS unsigned*)(sm + QL_O + 9216);
    bf16x8 wq[2][16], wo[8][4];
    const bf16x8 one = ones8();
    f32x4 sq[2];
    __syncthreads();
    { unsigned k0 = 0; if (w == 0) wait_words(ctl, C_PA + h, 1, 1, k0, lane, 8); __syncthreads(); }
    stage_w(p.Wq, 4096, 0, h * 128, 512, ldsb, w, lane); VMW(0); __syncthreads();
#pragma unroll
    for (int t = 0; t < 2; ++t) { sq[t] = Z4;
#pragma unroll
        for (int ks = 0; ks < 16; ++ks) { wq[t][ks] = wfrag(sm, ks * 32, w * 32 + t * 16, c, g); sq[t] = MFMA16(wq[t][ks], one, sq[t]); } }
    __syncthreads();
    for (int q = 0; q < 4; ++q) stage_w(p.Wo + (size_t)h * 128 * D, D, 0, q * 128, 128, ldsb + q * 32768, w, lane);
    VMW(0); __syncthreads();
#pragma unroll
    for (int t = 0; t < 8; ++t)
#pragma unroll
        for (int ks = 0; ks < 4; ++ks) wo[t][ks] = wfrag(sm + w * 32768, ks * 32, t * 16, c, g);
    __syncthreads();
    Known2 kg = {0, 0, 0};
    unsigned kkv = 0, kr = 0;
    bool pre = false;
    int xb = 0;
    for (int nn = par; nn < NL * NSEG; nn += 2) {
        const int layer = nn >> 6, s = nn & 63;
        const unsigned ob = nn * 262144 + h * 8192;
        if (s < 2) { kkv = 0; kr = 0; }
        if (!pre) { wait2_wg(ctl, C_PG, 16, 8, nn, kg, lane, w, slot, 2); stage_x32(rws, OFF_XA16 + nn * 32768, ldsb + QL_X + xb, w, lane); }
        VMW(0); __syncthreads();
        const int n2 = nn + 2;
        unsigned look = ~0u;
        if (w == 0 && n2 < NL * NSEG && lane < 8) look = __hip_atomic_load(ctl + C_PG + ((n2 >> 6) * 2 + (n2 & 1)) * 16 + lane, RLX_AGENT);
        f32x4 aq[2][2] = {{Z4, Z4}, {Z4, Z4}};
        proj32<true>(sm + QL_X + xb, wq, aq, c, g);
        wait_words_wg(ctl, C_FKV + (layer * 32 + h) * 16, 2, s + 1, kkv, lane, w, slot, 2);
        u32x2 st2[2];
#pragma unroll
        for (int rt = 0; rt < 2; ++rt) st2[rt] = gld8(rws, (rt * 16 + c) * 8, OFF_ST + (nn * 32 + h) * 256);
        dma16(rws, lane * 16, OFF_KX + ob + w * 2048, ldsb + QL_K + w * 2048); dma16(rws, lane * 16, OFF_KX + ob + w * 2048 + 1024, ldsb + QL_K + w * 2048 + 1024);
        const u32x4 vx0 = gld16(rws, lane * 16, OFF_VX + ob + w * 2048), vx1 = gld16(rws, lane * 16, OFF_VX + ob + w * 2048 + 1024);
#pragma unroll
        for (int rt = 0; rt < 2; ++rt) {
            const float mu = __uint_as_float(st2[rt][0]), rs = __uint_as_float(st2[rt][1]);
            const f32x4 y0 = (aq[0][rt] - mu * sq[0]) * rs, y1 = (aq[1][rt] - mu * sq[1]) * rs;
            ldsw(sm + QL_Q + (rt * 4 + w) * 1024 + lane * 16, (u32x4){cvtpk(y0[0], y0[1]), cvtpk(y0[2], y0[3]), cvtpk(y1[0], y1[1]), cvtpk(y1[2], y1[3])});
        }
        if (w == 0) { const bool rdy = n2 < NL * NSEG && __all(look >= (unsigned)((n2 & 63) >> 1) + 1); if (lane == 0) slot[1] = rdy; }
        LBAR();
        pre = __builtin_amdgcn_readfirstlane(slot[1]) != 0;
        if (pre) stage_x32(rws, OFF_XA16 + (nn + 2) * 32768, ldsb + QL_X + (xb ^ 32768), w, lane);
        xb ^= 32768;
        if (pre) VMW(8); else VMW(0);
        LBAR();
        f32x4 sc[2][3] = {{Z4, Z4, Z4}, {Z4, Z4, Z4}};
        bf16x8 qf[2][4];
        {
            bf16x8 k0[4], k1[4];
#pragma unroll
            for (int ks = 0; ks < 4; ++ks) { k0[ks] = asbf8(ldsr(sm + QL_K + ks * 1024 + lane * 16)); k1[ks] = asbf8(ldsr(sm + QL_K + 4096 + ks * 1024 + lane * 16));
                qf[0][ks] = asbf8(ldsr(sm + QL_Q + ks * 1024 + lane * 16)); qf[1][ks] = asbf8(ldsr(sm + QL_Q + (4 + ks) * 1024 + lane * 16)); }
#pragma unroll
            for (int ks = 0; ks < 4; ++ks)
#pragma unroll
                for (int qt = 0; qt < 2; ++qt) { sc[qt][1] = MFMA16(k0[ks], qf[qt][ks], sc[qt][1]); sc[qt][2] = MFMA16(k1[ks], qf[qt][ks], sc[qt][2]); }
        }
        wait_words_wg(ctl, C_FR + (layer * 32 + h) * 16, 4, s + 1, kr, lane, w, slot, s >= NSEG - 2 ? 0 : 1);
        const unsigned xfb = OFF_XF + (nn * 32 + h) * 4096;
        u32x4 km[4];
#pragma unroll
        for (int ks = 0; ks < 4; ++ks) km[ks] = gld16(rws, lane * 16, xfb + ks * 1024);
        const u32x2 ym = gld8(rws, lane * 8, OFF_VM + (nn * 32 + h) * 2048 + w * 512);
#pragma unroll
        for (int ks = 0; ks < 4; ++ks)
#pragma unroll
            for (int qt = 0; qt < 2; ++qt) sc[qt][0] = MFMA16(asbf8(km[ks]), qf[qt][ks], sc[qt][0]);
        const u32x4 ym4 = {ym[0], ym[1], 0u, 0u};
#pragma unroll
        for (int qt = 0; qt < 2; ++qt) {
            const int qi = qt * 16 + c;
            float m = -INFINITY;
#pragma unroll
            for (int r = 0; r < 4; ++r) {
                if (4 * g + r > qi) sc[qt][1][r] = -INFINITY;
                if (16 + 4 * g + r > qi) sc[qt][2][r] = -INFINITY;
                m = fmaxf(m, fmaxf(sc[qt][0][r], fmaxf(sc[qt][1][r], sc[qt][2][r])));
            }
            m = xmax16(m); m = xmax32(m);
            float l = 0, e[3][4];
#pragma unroll
            for (int i = 0; i < 3; ++i)
#pragma unroll
                for (int r = 0; r < 4; ++r) { e[i][r] = __builtin_amdgcn_exp2f(sc[qt][i][r] - m); l += (i == 0 && g >= 2) ? 0.f : e[i][r]; }
            l = xsum16(l); l = xsum32(l);
            const float il = __builtin_amdgcn_rcpf(l);
            const unsigned a0 = cvtpk(e[0][0], e[0][1]), a1 = cvtpk(e[0][2], e[0][3]);
            const u32x4 pb0 = g < 2 ? (u32x4){a0, a1, 0u, 0u} : (u32x4){0u, 0u, 0u, 0u}, pb1 = g >= 2 ? (u32x4){a0, a1, 0u, 0u} : (u32x4){0u, 0u, 0u, 0u};
            const u32x4 px = {cvtpk(e[1][0], e[1][1]), cvtpk(e[1][2], e[1][3]), cvtpk(e[2][0], e[2][1]), cvtpk(e[2][2], e[2][3])};
#pragma unroll
            for (int t = 0; t < 2; ++t) {
                f32x4 o = Z4;
                o = MFMA16(asbf8(t == 0 ? vx0 : vx1), asbf8(px), o); o = MFMA16(asbf8(ym4), asbf8(t == 0 ? pb0 : pb1), o);
                *(LAS u32x2*)(sm + QL_O + qi * 272 + (w * 32 + t * 16 + 4 * g) * 2) = (u32x2){cvtpk(o[0] * il, o[1] * il), cvtpk(o[2] * il, o[3] * il)};
            }
        }
        LBAR();
        bf16x8 of[2][4];
#pragma unroll
        for (int ks = 0; ks < 4; ++ks)
#pragma unroll
            for (int rt = 0; rt < 2; ++rt) of[rt][ks] = asbf8(ldsr(sm + QL_O + (rt * 16 + c) * 272 + (ks * 32 + g * 8) * 2));
#pragma unroll
        for (int j = 0; j < 4; ++j) {
            f32x4 a[2][2] = {{Z4, Z4}, {Z4, Z4}};
#pragma unroll
            for (int ks = 0; ks < 4; ++ks)
#pragma unroll
                for (int rt = 0; rt < 2; ++rt) { a[rt][0] = MFMA16(wo[2 * j][ks], of[rt][ks], a[rt][0]); a[rt][1] = MFMA16(wo[2 * j + 1][ks], of[rt][ks], a[rt][1]); }
#pragma unroll
            for (int rt = 0; rt < 2; ++rt)
                gst16((u32x4){cvtpk(a[rt][0][0], a[rt][0][1]), cvtpk(a[rt][0][2], a[rt][0][3]), cvtpk(a[rt][1][0], a[rt][1][1]), cvtpk(a[rt][1][2], a[rt][1][3])}, rws, lane * 16,
                      OFF_PB + ((s * 16 + w * 4 + j) * 32 + h) * 2048 + rt * 1024);
        }
        publish(ctl, C_PQ + (layer * 2 + par) * 32 + h, (s >> 1) + 1);
    }
}

DI void role_red(const Params& p, rsrc_t rws, gu32* ctl, LAS char* sm, int cb) {
    Known2 kq = {0, 0, 0};
    for (int nn = 0; nn < NL * NSEG; ++nn) if (!red_by_g(nn & 63)) red_task(p, rws, ctl, sm, cb, nn, kq, C_PR + cb, 80 + cb);
}

constexpr int RL_W = 0, RL_M = 131072, RL_QK = RL_M + 8192, RL_O = RL_QK + 4096, RL_KX = RL_O + 2560, RL_VX = RL_KX + 8192, RL_END = RL_VX + 8192;
static_assert(RL_END <= LDS_BYTES, "lds");
DI void put8(u32x32& W, int i, s16x4 f0, s16x4 f1, s16x4 f2, s16x4 f3) {
    const u32x2 a = __builtin_bit_cast(u32x2, f0), b = __builtin_bit_cast(u32x2, f1), c = __builtin_bit_cast(u32x2, f2), d = __builtin_bit_cast(u32x2, f3);
    W[8 * i + 0] = a[0]; W[8 * i + 1] = a[1]; W[8 * i + 2] = b[0]; W[8 * i + 3] = b[1]; W[8 * i + 4] = c[0]; W[8 * i + 5] = c[1]; W[8 * i + 6] = d[0]; W[8 * i + 7] = d[1];
}
DI void role_r(const Params& p, rsrc_t rws, gu32* ctl, LAS char* sm, int h) {
    const int tid = threadIdx.x, lane = tid & 63, c = lane & 15, g = lane >> 4;
    const int w = __builtin_amdgcn_readfirstlane(tid >> 6);
    const int q4 = c >> 2, p4 = c & 3;
    const unsigned l16 = lane * 16, ldsb = (unsigned)(uintptr_t)sm;
    {
        const u32x4 z = {~0u, ~0u, ~0u, ~0u};
        for (int i = 0; i < 2; ++i) for (int n = 0; n < 4; ++n) gst16(z, rws, l16, OFF_PM + i * 524288 + h * 16384 + w * 4096 + n * 1024);
        for (int ly = 0; ly < 2; ++ly) for (int i = 0; i < 2; ++i) for (int n = 0; n < 4; ++n) { const int sl = h * 2 + i;
            if (sl > 0) gst16(z, rws, l16, (ly == 0 ? OFF_PM : OFF_PM1) + MB + sl * 16384 + w * 4096 + n * 1024); }
    }
    u32x32 WO0, WO1, WO2, WO3, WQ0, WQ1, WQ2, WQ3, WK0, WK1, WV0, WV1;
    auto krow = [&](int rho) { return 32 * (q4 & 1) + 16 * (g & 1) + 8 * (rho >> 1) + 4 * (g >> 1) + 2 * (rho & 1) + (q4 >> 1); };
    auto erow = [&](int rho) { return 32 * (rho >> 1) + 16 * (g >> 1) + 8 * (q4 & 1) + 4 * (g & 1) + 2 * (rho & 1) + (q4 >> 1); };
    for (int q = 0; q < 4; ++q) stage_w(p.Wo + (size_t)h * 128 * D, D, 0, q * 128, 128, ldsb + q * 32768, w, lane);
    VMW(0); __syncthreads();
#pragma unroll
    for (int t = 0; t < 8; ++t)
#pragma unroll
        for (int ks = 0; ks < 2; ++ks) {
            s16x4 f[4];
#pragma unroll
            for (int rho = 0; rho < 4; ++rho) f[rho] = trread(wimg(sm + w * 32768, ks * 64 + erow(rho), t * 16 + 4 * p4));
            const int i = t * 2 + ks;
            put8(i < 4 ? WO0 : i < 8 ? WO1 : i < 12 ? WO2 : WO3, i & 3, f[0], f[1], f[2], f[3]);
        }
    __syncthreads();
    stage_w(p.Wq, 4096, 0, h * 128, 512, ldsb, w, lane); VMW(0); __syncthreads();
#pragma unroll
    for (int ks = 0; ks < 8; ++ks)
#pragma unroll
        for (int t = 0; t < 2; ++t) {
            s16x4 f[4];
#pragma unroll
            for (int rho = 0; rho < 4; ++rho) f[rho] = trread(wimg(sm, ks * 64 + krow(rho), 32 * w + 16 * t + 4 * p4));
            const int i = ks * 2 + t;
            put8(i < 4 ? WQ0 : i < 8 ? WQ1 : i < 12 ? WQ2 : WQ3, i & 3, f[0], f[1], f[2], f[3]);
        }
    __syncthreads();
    stage_w(p.Wk, 4096, 256, h * 128, 256, ldsb, w, lane); stage_w(p.Wv, 4096, 0, h * 128, 256, ldsb + 65536, w, lane); VMW(0); __syncthreads();
#pragma unroll
    for (int m = 0; m < 2; ++m)
#pragma unroll
        for (int kq = 0; kq < 4; ++kq)
#pragma unroll
            for (int t = 0; t < 2; ++t) {
                s16x4 f[4];
#pragma unroll
                for (int rho = 0; rho < 4; ++rho) f[rho] = trread(wimg(sm + m * 65536, kq * 64 + krow(rho), 32 * w + 16 * t + 4 * p4));
                const int i = kq * 2 + t;
                if (m == 0) put8(i < 4 ? WK0 : WK1, i & 3, f[0], f[1], f[2], f[3]); else put8(i < 4 ? WV0 : WV1, i & 3, f[0], f[1], f[2], f[3]);
            }
    __syncthreads();
    stage_w(p.Wk, 4096, 0, h * 128, 64, ldsb + RL_M, w, lane);
    for (int kk = 0; kk < 8; ++kk) {
        if (kk + 1 < 8) { stage_w(kk + 1 < 4 ? p.Wk : p.Wv, 4096, (kk + 1) * 64, h * 128, 64, ldsb + RL_M + ((kk + 1) & 1) * 16384, w, lane); VMW(4); } else VMW(0);
        __syncthreads();
        for (int t = 0; t < 2; ++t) {
            s16x4 f[4];
#pragma unroll
            for (int rho = 0; rho < 4; ++rho) f[rho] = trread(wimg(sm + RL_M + (kk & 1) * 16384, krow(rho), 32 * w + 16 * t + 4 * p4));
            LAS char* dst = sm + RL_W + w * 32768 + (kk * 2 + t) * 2048 + lane * 16;
            ldsw(dst, asu4(cat8(f[0], f[1]))); ldsw(dst + 1024, asu4(cat8(f[2], f[3])));
        }
        LBAR();
    }
    asm volatile("s_waitcnt vmcnt(0)" ::: "memory");
    __syncthreads();
    if (tid == 0) __hip_atomic_fetch_add(ctl + C_RBAR, 1u, RLX_AGENT);
    unsigned kb = 0, par0 = 0, par1 = 0;
    wait_words(ctl, C_RBAR, 1, 32, kb, lane); wait_words(ctl, C_PAR, 1, 1, par0, lane); wait_words(ctl, C_PAR + 16, 1, 1, par1, lane);
    par0 = par0 == 2u; par1 = par1 == 2u;
#pragma unroll 1
    for (int layer = 0; layer < NL; ++layer) {
        const int ln = fresh_lane(), td = w * 64 + ln;
        __syncthreads();
        for (int i = td; i < 4096; i += 256) {
            const int row = i >> 9, k = i & 511;
            const int addr = (k >> 6) * 1024 + ((k >> 4) & 1) * 512 + ((k >> 3) & 1) * 256 + (row >> 2) * 128 + ((k >> 2) & 1) * 64 + (k & 3) * 16 + ((k >> 5) & 1) * 8 + (row & 3) * 2;
            *(LAS u16*)(sm + RL_M + addr) = p.mem0[i];
        }
        { unsigned kk = 0; wait_words(ctl, C_FKV + (layer * 32 + h) * 16, 2, 1, kk, ln, 1); }
        const unsigned kxb = OFF_KX + layer * 64 * 262144, vxb = OFF_VX + layer * 64 * 262144;
        for (int n = 0; n < 2; ++n) {
            dma16(rws, ln * 16, kxb + h * 8192 + w * 2048 + n * 1024, ldsb + RL_KX + w * 2048 + n * 1024);
            dma16(rws, ln * 16, vxb + h * 8192 + w * 2048 + n * 1024, ldsb + RL_VX + w * 2048 + n * 1024);
        }
        VMW(0); __syncthreads();
        const unsigned sPB = OFF_PM + par0 * 8192, sMB = (layer == 0 ? OFF_PM + MB + par0 * 8192 : OFF_PM1 + MB + par1 * 8192);
        const unsigned sFK = OFF_CTL + (C_FKV + (layer * 32 + h) * 16) * 4, sFR = OFF_CTL + (C_FR + (layer * 32 + h) * 16 + w) * 4;
        asm volatile(R_ASM
            :
            : "{a[0:31]}"(WQ0), "{a[32:63]}"(WQ1), "{a[64:95]}"(WQ2), "{a[96:127]}"(WQ3), "{a[128:159]}"(WK0), "{a[160:191]}"(WK1), "{a[192:223]}"(WV0), "{a[224:255]}"(WV1),
              "{v[128:159]}"(WO0), "{v[160:191]}"(WO1), "{v[192:223]}"(WO2), "{v[224:255]}"(WO3),
              "{s[40:43]}"(rws), "{s76}"(h), "{s77}"(w), "{s78}"(ldsb), "{s79}"(sPB), "{s80}"(sMB), "{s81}"(kxb), "{s82}"(vxb),
              "{s83}"(OFF_XF + layer * 64 * 131072), "{s84}"(OFF_VM + layer * 64 * 65536), "{s85}"(p.d1), "{s86}"(p.d2), "{s87}"(p.dd), "{s32}"(sFK), "{s33}"(sFR), "{s35}"(p.de)
            : "memory", "vcc", "scc",
              "v0","v1","v2","v3","v4","v5","v6","v7","v8","v9","v10","v11","v12","v13","v15","v16","v17","v18","v19","v20","v21","v22","v23","v24","v25","v26","v27","v28","v29","v30","v31",
              "v32","v33","v34","v35","v36","v37","v38","v39","v40","v41","v42","v43","v44","v45","v46","v47","v48","v49","v50","v51","v52","v53","v54","v55","v56","v57","v58","v59","v60","v61","v62","v63",
              "v64","v65","v66","v67","v68","v69","v70","v71","v72","v73","v74","v75","v76","v77","v78","v79","v80","v81","v82","v83","v84","v85","v86","v87","v88","v89","v90","v91","v92","v93","v94","v95",
              "v96","v97","v98","v99","v100","v101","v102","v103","v104","v105","v106","v107","v108","v109","v110","v111","v112","v113","v114","v115","v116","v117","v118","v119","v120","v121","v122","v123","v124","v125","v126","v127",
              "s34","s36","s37","s38","s39",
              "s44","s45","s46","s47","s48","s49","s50","s51","s52","s53","s54","s55","s56","s57","s58","s59","s60","s61","s62","s63","s64","s65","s66","s67","s68","s69","s70","s71","s72","s73","s74","s75");
    }
}

__global__ void __launch_bounds__(256, 1) mega(Params p) {
    extern __shared__ __attribute__((aligned(16))) char dsm[];
    LAS char* sm = (LAS char*)dsm;
    gu32* ctl = (gu32*)(p.ws + OFF_CTL);
    const rsrc_t rws = __builtin_amdgcn_make_buffer_rsrc(p.ws, 0, WS_SPAN, 0x00020000);
    if (threadIdx.x == 0) {
        const unsigned xcc = __builtin_amdgcn_s_getreg(20 | (3 << 11)) & 0xf;
        const unsigned rank = __hip_atomic_fetch_add(ctl + C_XCNT + xcc * 16, 1u, RLX_AGENT);
        int slot = (xcc < 8 && rank < 32) ? (int)(xcc * 32 + rank) : -1;
        if (slot < 0) {
            int k = (int)__hip_atomic_fetch_add(ctl + C_WCNT, 1u, RLX_AGENT);
            unsigned sp = 0;
            for (;;) {
                unsigned tot = 0, cnt[8];
                for (int x = 0; x < 16; ++x) { const unsigned v = __hip_atomic_load(ctl + C_XCNT + x * 16, RLX_AGENT); tot += v; if (x < 8) cnt[x] = min(v, 32u); }
                if (tot >= gridDim.x) {
                    for (int x = 0; x < 8 && slot < 0; ++x) { const int miss = 32 - (int)cnt[x]; if (k < miss) slot = x * 32 + (int)cnt[x] + k; else k -= miss; }
                    break;
                }
                __builtin_amdgcn_s_sleep(3);
                if (++sp > SPIN_CAP) { __hip_atomic_store(ctl + C_TMO, 1u, RLX_AGENT); break; }
            }
        }
        int role = 1000;
        if (slot >= 0) { const int x = slot >> 5, r = slot & 31;
            role = x < 4 ? (r < 8 ? -1 - (x * 8 + r) : (r < 24 ? x * 16 + r - 8 : (r < 28 ? 64 + x * 4 + r - 24 : 80 + x * 4 + r - 28))) : 96 + (x - 4) * 32 + r;
            if (role >= 80 && role < 96) role += 64; else if (role >= 144 && role < 160) role -= 64;
        }
        ((LAS int*)sm)[1] = slot;
        *(LAS int*)sm = role;
    }
    __syncthreads();
    const int role = __builtin_amdgcn_readfirstlane(*(LAS int*)sm), slot = __builtin_amdgcn_readfirstlane(((LAS int*)sm)[1]);
    __syncthreads();
    if (role < 0) { role_r(p, rws, ctl, sm, -1 - role); }
    else if (role < 224) {
        { const int x = slot >> 5, r = slot & 31;
          if (x < 4 && r < 24) role_t1_init(p, rws, ctl, sm, x * 4 + ((r - 8) >> 2), 8 + (r & 3));
          else if (x >= 4) role_t1_init(p, rws, ctl, sm, (x - 4) * 4 + (r >> 3), r & 7); }
        __syncthreads();
        if (role < 64) { role_att(p, rws, ctl, sm, role >> 1, role & 1); }
        else if (role < 80) { role_g(p, rws, ctl, sm, (role - 64) >> 3, role & 7); }
        else if (role < 96) { role_red(p, rws, ctl, sm, role - 80); }
        else if (role < 160) { { if (role & 1) role_kv<1>(p, rws, ctl, sm, (role - 96) >> 1); else role_kv<0>(p, rws, ctl, sm, (role - 96) >> 1); } }
        else { role_qo(p, rws, ctl, sm, (role - 160) >> 1, role & 1); }
    }
}

extern "C" void kernel_launch(void* const* d_in, const int* in_sizes, int n_in, void* d_out, int out_size, void* d_ws, size_t ws_size, hipStream_t stream) {
    Params p;
    p.x = (const u16*)d_in[0]; p.wqkv = (const u16*)d_in[1]; p.wo = (const u16*)d_in[2];
    p.mem0 = (const u16*)d_in[4]; p.Wq = (const u16*)d_in[5]; p.Wk = (const u16*)d_in[6]; p.Wv = (const u16*)d_in[7]; p.Wo = (const u16*)d_in[8];
    p.out = (u16*)d_out; p.ws = (char*)(((uintptr_t)d_ws + 0x1fffff) & ~(uintptr_t)0x1fffff);
    p.d1 = D1; p.d2 = D2; p.dd = DD; p.de = DE;
    (void)hipFuncSetAttribute((const void*)mega, hipFuncAttributeMaxDynamicSharedMemorySize, LDS_BYTES);
    (void)hipMemsetAsync(p.ws + OFF_CTL, 0, CTL_BYTES, stream);
    mega<<<256, 256, LDS_BYTES, stream>>>(p);
}
```
